```python
import torch

def _softmax(x, axis=-1):
    return torch.softmax(x, dim=axis)
def _gelu(x):
    return torch.nn.functional.gelu(x, approximate="tanh")
def _silu(x):
    return torch.nn.functional.silu(x)
def _sigmoid(x):
    return torch.sigmoid(x)
def _layernorm(x, axis=-1, eps=1e-5):
    m = x.mean(dim=axis, keepdim=True)
    v = x.var(dim=axis, keepdim=True, unbiased=False)
    return (x - m) / torch.sqrt(v + eps)
def _scatter_add(tgt, idx, src, axis=0):
    return tgt.clone().index_add_(axis, idx, src)
def _segment_sum(src, seg, n):
    out = torch.zeros((n,) + src.shape[1:], dtype=src.dtype)
    return out.index_add_(0, seg, src)
def _pad(x, pw):
    flat = tuple(v for pair in reversed(pw) for v in pair)
    return torch.nn.functional.pad(x, flat)

kernel_name = 'gated_mix__softmax_full_mha__dense_activation_silu__prompt'

def setup_inputs():

    _w = (lambda a: (lambda t: t.to(torch.bfloat16) if t.is_floating_point() else t)(torch.as_tensor(a)))
    def _A_setup():
        B, S, D = 1, 2048, 448
        return {
            "x": _w((lambda *s: torch.randn(*s, dtype=torch.bfloat16) * 0.05)(B, S, D)),
            "wqkv": _w((lambda *s: torch.randn(*s, dtype=torch.bfloat16) * 0.8)(D, 3 * D)),
            "wo": _w((lambda *s: torch.randn(*s, dtype=torch.bfloat16) * 0.05)(D, D)),
        }
    _A_in = _A_setup()

    def _B_setup():
        B, S, D, F = 1, 2048, 448, 1792
        return {
            "x": _w((lambda *s: torch.randn(*s, dtype=torch.bfloat16) * 0.05)(B, S, D)),
            "w1": _w((lambda *s: torch.randn(*s, dtype=torch.bfloat16) * 3.2)(D, F) * (D ** -0.5)),
            "w2": _w((lambda *s: torch.randn(*s, dtype=torch.bfloat16) * 0.8)(F, D) * (F ** -0.5)),
        }
    _B_in = _B_setup()

    return {
        "A_x": _A_in["x"],
        "A_wqkv": _A_in["wqkv"],
        "A_wo": _A_in["wo"],
        "B_x": _B_in["x"],
        "B_w1": _B_in["w1"],
        "B_w2": _B_in["w2"],
        "gate_w": _w((lambda *s: torch.randn(*s, dtype=torch.bfloat16) * 0.05)(448) * 16.0),
    }

def reference(A_x, A_wqkv, A_wo, B_x, B_w1, B_w2, gate_w):
    def _A_ref(x, wqkv, wo):
        B, S, D = x.shape
        H = 16; Dh = D // H
        qkv = (lambda x, *d: x.permute(*d))((x @ wqkv).reshape(B, S, 3, H, Dh), 2, 0, 3, 1, 4)
        q, k, v = qkv[0], qkv[1], qkv[2]
        scale = Dh ** -0.5
        scores = torch.einsum("bhqd,bhkd->bhqk", q, k) * scale
        p = _softmax(scores, axis=-1)
        o = torch.einsum("bhqk,bhkd->bhqd", p, v)
        return (lambda x, *d: x.permute(*d))(o, 0, 2, 1, 3).reshape(B, S, D) @ wo

    def _B_ref(x, w1, w2):
        h = torch.einsum("bsd,df->bsf", x, w1)
        h = _silu(h)
        out = torch.einsum("bsf,fd->bsd", h, w2)
        return out

    x = A_x
    g = _sigmoid((x * gate_w).sum(-1, keepdims=True))
    a = _A_ref(x, A_wqkv, A_wo)
    b = _B_ref(x, B_w1, B_w2)
    return g * a + (1 - g) * b
```

```cpp
#include <hip/hip_runtime.h>
#include <hip/hip_bf16.h>
#include <stdint.h>

typedef __attribute__((ext_vector_type(8))) short bf16x8;
typedef __attribute__((ext_vector_type(4))) short s16x4;
typedef __attribute__((ext_vector_type(16))) float f32x16;
typedef __attribute__((ext_vector_type(4))) float f32x4;
typedef __attribute__((ext_vector_type(4))) unsigned u32x4;
typedef __attribute__((ext_vector_type(2))) unsigned u32x2;
typedef unsigned short u16;
#define LAS __attribute__((address_space(3)))
typedef LAS const char* lds_cptr;
typedef LAS char* lds_ptr;
#define DI __device__ __forceinline__
#define MFMA(a, b, c) __builtin_amdgcn_mfma_f32_32x32x16_bf16((a), (b), (c), 0, 0, 0)

constexpr int SEQ = 2048, DM = 448, NH = 16, DH = 28, FF = 1792, NQKV = 1344;
constexpr size_t WS_Q = 0;
constexpr size_t WS_KV = WS_Q + (2u << 20);
constexpr size_t WS_H = WS_KV + (4u << 20);
constexpr size_t WS_O = WS_H + (8u << 20);
constexpr size_t WS_B = WS_O + (2u << 20);
constexpr size_t WS_G = WS_B + (4u << 20);

DI unsigned cvtpk(float lo, float hi) {
    typedef __attribute__((ext_vector_type(2))) float f2; typedef __attribute__((ext_vector_type(2))) __bf16 b2;
    f2 v = {lo, hi}; b2 r = __builtin_convertvector(v, b2); return __builtin_bit_cast(unsigned, r);
}
DI float bf_lo(unsigned w) { return __uint_as_float(w << 16); }
DI float bf_hi(unsigned w) { return __uint_as_float(w & 0xffff0000u); }
DI float bfround(float x) { return bf_lo(cvtpk(x, 0.f)); }
DI void glds16(const void* g, unsigned lds_base) {
    unsigned sv;
    asm volatile("s_mov_b32 %0, m0\n\ts_mov_b32 m0, %2\n\ts_nop 0\n\tglobal_load_lds_dwordx4 %1, off\n\ts_mov_b32 m0, %0"
                 : "=&s"(sv) : "v"(g), "s"(lds_base) : "memory");
}
#define RFL(x) __builtin_amdgcn_readfirstlane(x)
DI bf16x8 lds128(lds_cptr p) { return *(const LAS bf16x8*)p; }
DI s16x4 ldstr(lds_cptr p) {
    typedef short v4i16_t __attribute__((ext_vector_type(4)));
    return __builtin_bit_cast(s16x4, __builtin_amdgcn_ds_read_tr16_b64_v4i16((LAS v4i16_t*)p));
}
DI bf16x8 trfrag(lds_cptr p0, lds_cptr p1) { s16x4 a = ldstr(p0), b = ldstr(p1); return __builtin_shufflevector(a, b, 0, 1, 2, 3, 4, 5, 6, 7); }
DI bf16x8 pack8(const f32x16& x, int s) {
    u32x4 p = {cvtpk(x[8 * s], x[8 * s + 1]), cvtpk(x[8 * s + 2], x[8 * s + 3]), cvtpk(x[8 * s + 4], x[8 * s + 5]), cvtpk(x[8 * s + 6], x[8 * s + 7])};
    return __builtin_bit_cast(bf16x8, p);
}
DI float swap32(float v) {
    auto r = __builtin_amdgcn_permlane32_swap(__float_as_uint(v), __float_as_uint(v), false, false);
    return (threadIdx.x & 32) ? __uint_as_float(r[0]) : __uint_as_float(r[1]);
}
template <int N> DI void wait_bar() { asm volatile("s_waitcnt vmcnt(%0) lgkmcnt(0)\n\ts_barrier" ::"n"(N) : "memory"); }
template <int N> DI void wait_vm() { asm volatile("s_waitcnt vmcnt(%0)" ::"n"(N) : "memory"); }
DI void bar_lds() { asm volatile("s_waitcnt lgkmcnt(0)\n\ts_barrier" ::: "memory"); }

constexpr int K1_X = 0, K1_W = 114688, K1_SLOT = 14336, K1_G = K1_W + 3 * K1_SLOT, K1_LDS = K1_G + 1024;

template <int CG>
DI void k1_body(const u16* __restrict__ x, const u16* __restrict__ wqkv, const u16* __restrict__ w1, const u16* __restrict__ gw,
                char* ws, char* lds, int h, int t, int rb, int w) {
    const int lane = threadIdx.x & 63, r = lane & 31, hh = lane >> 5;
    const unsigned l0 = (unsigned)(uintptr_t)lds;
    const int cs = (h * DH) & ~7;
    const u16* xs0 = x + (size_t)(t * 128 + 16 * w + (lane >> 3)) * DM + (((lane & 7) ^ ((lane >> 4) & 7)) * 8);
    const u16* xs1 = x + (size_t)(t * 128 + 16 * w + 8 + (lane >> 3)) * DM + (((lane & 7) ^ ((4 + (lane >> 4)) & 7)) * 8);
    const u16* wq = wqkv + (size_t)(16 * rb + (lane >> 2)) * NQKV + cs + (lane & 3) * 8 + (CG ? 2 * DM : 0);
    const u16* wf = w1 + (size_t)(16 * rb + (lane >> 2)) * FF + 112 * h + (lane & 3) * 8 + (CG ? 64 : 0);
    const u16* wf3 = w1 + (size_t)(16 * rb + ((lane & 31) >> 1)) * FF + 112 * h + 96 + (lane & 1) * 8;
    auto dma_x = [&](int kc) {
        glds16(xs0 + kc * 64, RFL(l0 + K1_X + kc * 16384 + w * 2048));
        glds16(xs1 + kc * 64, RFL(l0 + K1_X + kc * 16384 + w * 2048 + 1024));
    };
    auto dma_w = [&](int s) {
        const unsigned sl = l0 + K1_W + (s % 3) * K1_SLOT + rb * 1024;
        if (s < 7) {
            if (CG == 0) { glds16(wq + (size_t)s * 64 * NQKV, RFL(sl)); glds16(wq + (size_t)s * 64 * NQKV + DM, RFL(sl + 4096)); }
            else glds16(wq + (size_t)s * 64 * NQKV, RFL(sl + 8192));
        } else {
            const size_t o = (size_t)(s - 7) * 64 * FF;
            if (CG == 0) { glds16(wf + o, RFL(sl)); glds16(wf + o + 32, RFL(sl + 4096)); }
            else {
                glds16(wf + o, RFL(sl + 8192));
                if (lane < 32) glds16(wf3 + o, RFL(sl - rb * 512 + 12288));
            }
        }
    };
    if (w == 7 && lane < 56) glds16(gw + lane * 8, RFL(l0 + K1_G));
    dma_x(0); dma_w(0); dma_x(1); dma_w(1); dma_x(2); dma_x(3);

    const int sw = (r >> 1) & 7;
    lds_cptr xb[4];
#pragma unroll
    for (int j = 0; j < 4; ++j) xb[j] = (lds_cptr)lds + K1_X + (32 * rb + r) * 128 + (((2 * j + hh) ^ sw) << 4);
    const int q4 = (lane & 15) >> 2, p4 = lane & 3, blk = (lane >> 4) & 1;
    lds_cptr wb = (lds_cptr)lds + K1_W + (8 * hh + q4) * 64 + blk * 32 + p4 * 8;
    lds_cptr wb3 = (lds_cptr)lds + K1_W + 12288 + (8 * hh + q4) * 32 + p4 * 8;
    lds_cptr gb = (lds_cptr)lds + K1_G + hh * 16;
    constexpr int A1 = CG ? 1 : 2, NST = CG ? 2 : 4;

    f32x16 c0 = {}, c1 = {};
#pragma unroll
    for (int s = 0; s < 7; ++s) {
        if (s == 0 || s == 1) wait_bar<6 + A1>();
        else if (s == 2 || s == 3) wait_bar<4 + A1>();
        else if (s == 4) wait_bar<2 + A1>();
        else if (s == 5) wait_bar<A1>();
        else wait_bar<2>();
        dma_w(s + 2);
        if (s < 3) dma_x(s + 4);
        lds_cptr wsl = wb + (s % 3) * K1_SLOT;
#pragma unroll
        for (int u = 0; u < 4; ++u) {
            const bf16x8 xf = lds128(xb[u] + s * 16384);
            if (CG == 0) {
                c0 = MFMA(trfrag(wsl + u * 1024, wsl + u * 1024 + 256), xf, c0);
                c1 = MFMA(trfrag(wsl + 4096 + u * 1024, wsl + 4096 + u * 1024 + 256), xf, c1);
            } else {
                c0 = MFMA(xf, trfrag(wsl + 8192 + u * 1024, wsl + 8192 + u * 1024 + 256), c0);
            }
        }
    }
    {
        const int rowblk = t * 4 + rb; const bool odd = h & 1;
        if (CG == 0) {
            const float qs = 0.18898223650461362f * 1.4426950408889634f;
            const bool padl = (hh != (int)odd) ? false : false; (void)padl;
            const bool pad_here = odd ? (hh == 0) : (hh == 1);
            float m = 0.f;
#pragma unroll
            for (int i = 0; i < 16; ++i) {
                c0[i] *= qs;
                const bool isp = pad_here && (odd ? (i < 4) : (i >= 12));
                m += isp ? 0.f : c0[i] * c1[i];
            }
            m += swap32(m);
            const float mh = bfround(m), mm = bfround(m - mh), ml = bfround(m - mh - mm);
#pragma unroll
            for (int i = 0; i < 4; ++i) {
                const float qv = i == 0 ? -mh : i == 1 ? -mm : i == 2 ? -ml : 0.f, kv = i < 3 ? 1.f : 0.f;
                if (pad_here && odd) { c0[i] = qv; c1[i] = kv; }
                if (pad_here && !odd) { c0[12 + i] = qv; c1[12 + i] = kv; }
            }
            bf16x8* qd = (bf16x8*)(ws + WS_Q) + ((size_t)(h * 64 + rowblk) * 2) * 64 + lane;
            bf16x8* kd = (bf16x8*)(ws + WS_KV) + ((size_t)(h * 64 + rowblk) * 4) * 64 + lane;
            kd[0] = pack8(c1, 0); kd[64] = pack8(c1, 1);
            qd[0] = pack8(c0, 0); qd[64] = pack8(c0, 1);
        } else {
            const bool isp = odd ? (r < 4) : (r >= 28); const float pv = (r == (odd ? 0 : 28)) ? 1.f : 0.f;
#pragma unroll
            for (int i = 0; i < 16; ++i) c0[i] = isp ? pv : c0[i];
            bf16x8* vd = (bf16x8*)(ws + WS_KV) + ((size_t)(h * 64 + rowblk) * 4 + 2) * 64 + lane;
            vd[0] = pack8(c0, 0); vd[64] = pack8(c0, 1);
        }
    }
    c0 = f32x16{}; c1 = f32x16{};
#pragma unroll
    for (int s = 7; s < 14; ++s) {
        if (s == 7 || s == 8) wait_bar<2 + NST>();
        else if (s == 13) wait_bar<0>();
        else wait_bar<2>();
        if (s + 2 < 14) dma_w(s + 2);
        lds_cptr wsl = wb + (s % 3) * K1_SLOT;
#pragma unroll
        for (int u = 0; u < 4; ++u) {
            const bf16x8 xf = lds128(xb[u] + (s - 7) * 16384);
            if (CG == 0) {
                c0 = MFMA(trfrag(wsl + u * 1024, wsl + u * 1024 + 256), xf, c0);
                c1 = MFMA(trfrag(wsl + 4096 + u * 1024, wsl + 4096 + u * 1024 + 256), xf, c1);
            } else {
                c0 = MFMA(trfrag(wsl + 8192 + u * 1024, wsl + 8192 + u * 1024 + 256), xf, c0);
                lds_cptr w3 = wb3 + (s % 3) * K1_SLOT + u * 512;
                const bf16x8 a3 = trfrag(w3, w3 + 128), gf = lds128(gb + ((s - 7) * 4 + u) * 32);
                c1 = MFMA(blk ? gf : a3, xf, c1);
            }
        }
    }
    {
        const int rowblk = t * 4 + rb;
        const float gz = c1[8];
#pragma unroll
        for (int i = 0; i < 16; ++i) {
            c0[i] = c0[i] * __builtin_amdgcn_rcpf(1.f + __builtin_amdgcn_exp2f(-1.4426950408889634f * c0[i]));
            if (CG == 0 || i < 8) c1[i] = c1[i] * __builtin_amdgcn_rcpf(1.f + __builtin_amdgcn_exp2f(-1.4426950408889634f * c1[i]));
        }
        bf16x8* hd = (bf16x8*)(ws + WS_H) + ((size_t)rowblk * 112 + h * 7 + CG * 4) * 64 + lane;
        hd[0] = pack8(c0, 0); hd[64] = pack8(c0, 1); hd[128] = pack8(c1, 0);
        if (CG == 0) hd[192] = pack8(c1, 1);
        if (CG == 1 && h == 0 && lane < 32)
            ((float*)(ws + WS_G))[rowblk * 32 + r] = __builtin_amdgcn_rcpf(1.f + __builtin_amdgcn_exp2f(-1.4426950408889634f * gz));
    }
}

__global__ __launch_bounds__(512, 2) void k1_proj(const u16* __restrict__ x, const u16* __restrict__ wqkv, const u16* __restrict__ w1,
                                                  const u16* __restrict__ gw, char* ws) {
    extern __shared__ __attribute__((aligned(16))) char lds[];
    const int xcd = blockIdx.x & 7, idx = blockIdx.x >> 3;
    const int t = 8 * (xcd & 1) + (idx & 7), h = 4 * (xcd >> 1) + (idx >> 3);
    const int w = RFL(threadIdx.x >> 6);
    if (w < 4) k1_body<0>(x, wqkv, w1, gw, ws, lds, h, t, w & 3, w);
    else k1_body<1>(x, wqkv, w1, gw, ws, lds, h, t, w & 3, w);
}

constexpr int K2_M = 0, K2_R = 65536, K2_MX = 131072, K2_FLAG = K2_MX + 4096, K2_LDS = K2_FLAG + 16;

DI void attn_body(char* ws, char* lds, int h, int t, int w) {
    const int lane = threadIdx.x & 63, r = lane & 31, hh = lane >> 5;
    const bool odd = h & 1;
    const bool pad_here = odd ? (hh == 0) : (hh == 1);
    const bf16x8* qsrc = (const bf16x8*)(ws + WS_Q) + ((size_t)(h * 64 + t * 4) * 2) * 64 + lane;
    const bf16x8* kv = (const bf16x8*)(ws + WS_KV) + ((size_t)(h * 64 + w * 8) * 4) * 64 + lane;
    bf16x8 qf[4][2];
#pragma unroll
    for (int i = 0; i < 4; ++i) { qf[i][0] = qsrc[i * 128]; qf[i][1] = qsrc[i * 128 + 64]; }
    for (int attempt = 0; attempt < 2; ++attempt) {
        f32x16 o[4];
#pragma unroll
        for (int i = 0; i < 4; ++i) o[i] = f32x16{};
        bf16x8 k0 = kv[0], k1 = kv[64], v0 = kv[128], v1 = kv[192];
#pragma unroll 1
        for (int kt = 0; kt < 8; ++kt) {
            const int nx = (kt < 7 ? kt + 1 : kt) * 256;
            const bf16x8 nk0 = kv[nx], nk1 = kv[nx + 64], nv0 = kv[nx + 128], nv1 = kv[nx + 192];
#pragma unroll
            for (int i = 0; i < 4; ++i) {
                f32x16 s = MFMA(k0, qf[i][0], f32x16{});
                s = MFMA(k1, qf[i][1], s);
#pragma unroll
                for (int j = 0; j < 16; ++j) s[j] = __builtin_amdgcn_exp2f(s[j]);
                o[i] = MFMA(v0, pack8(s, 0), o[i]);
                o[i] = MFMA(v1, pack8(s, 1), o[i]);
            }
            k0 = nk0; k1 = nk1; v0 = nv0; v1 = nv1;
        }
        LAS bf16x8* mb = (LAS bf16x8*)((lds_ptr)lds + K2_M) + lane;
#pragma unroll
        for (int i = 0; i < 4; ++i)
            if (i != w) { mb[(w * 8 + i * 2) * 64] = pack8(o[i], 0); mb[(w * 8 + i * 2 + 1) * 64] = pack8(o[i], 1); }
        if (threadIdx.x == 0) *(LAS unsigned*)((lds_ptr)lds + K2_FLAG) = 0u;
        bar_lds();
        if (w < 4) {
            f32x16 acc = w == 0 ? o[0] : w == 1 ? o[1] : w == 2 ? o[2] : o[3];
            bf16x8 id0, id1;
#pragma unroll
            for (int j = 0; j < 8; ++j) {
                const int kr = 8 * (j >> 2) + 4 * hh + (j & 3);
                id0[j] = (r == kr) ? (short)0x3f80 : (short)0; id1[j] = (r == kr + 16) ? (short)0x3f80 : (short)0;
            }
#pragma unroll
            for (int p = 1; p < 8; ++p) {
                const int src = (w + p) & 7;
                acc = MFMA(id0, mb[(src * 8 + w * 2) * 64], acc);
                acc = MFMA(id1, mb[(src * 8 + w * 2 + 1) * 64], acc);
            }
            float l = odd ? acc[0] : acc[12];
            const float lo = swap32(l);
            l = pad_here ? l : lo;
            float amax = 0.f;
#pragma unroll
            for (int i = 0; i < 16; ++i) amax = fmaxf(amax, fabsf(acc[i]));
            const bool bad = !(l > 1e-20f && l < 1e30f && amax < 1e36f);
            const float rl = __builtin_amdgcn_rcpf(l);
#pragma unroll
            for (int i = 0; i < 16; ++i) {
                const bool isp = pad_here && (odd ? (i < 4) : (i >= 12));
                acc[i] = isp ? 0.f : acc[i] * rl;
            }
            bf16x8* od = (bf16x8*)(ws + WS_O) + ((size_t)(t * 4 + w) * 32 + h * 2) * 64 + lane;
            od[0] = pack8(acc, 0); od[64] = pack8(acc, 1);
            if (__any(bad) && lane == 0) *(LAS unsigned*)((lds_ptr)lds + K2_FLAG) = 1u;
        }
        bar_lds();
        const unsigned redo = RFL(*(volatile LAS unsigned*)((lds_ptr)lds + K2_FLAG));
        if (!redo || attempt) break;
        float mx[4];
#pragma unroll
        for (int i = 0; i < 4; ++i) mx[i] = -INFINITY;
#pragma unroll 1
        for (int kt = 0; kt < 8; ++kt) {
            const bf16x8 a0 = kv[kt * 256], a1 = kv[kt * 256 + 64];
#pragma unroll
            for (int i = 0; i < 4; ++i) {
                f32x16 s = MFMA(a0, qf[i][0], f32x16{});
                s = MFMA(a1, qf[i][1], s);
#pragma unroll
                for (int j = 0; j < 16; ++j) mx[i] = fmaxf(mx[i], s[j]);
            }
        }
        LAS float* mxb = (LAS float*)((lds_ptr)lds + K2_MX);
#pragma unroll
        for (int i = 0; i < 4; ++i) { mx[i] = fmaxf(mx[i], swap32(mx[i])); if (hh == 0) mxb[w * 128 + i * 32 + r] = mx[i]; }
        bar_lds();
#pragma unroll
        for (int i = 0; i < 4; ++i) {
            float d = mxb[i * 32 + r];
#pragma unroll
            for (int p = 1; p < 8; ++p) d = fmaxf(d, mxb[p * 128 + i * 32 + r]);
            u32x4 f = __builtin_bit_cast(u32x4, odd ? qf[i][0] : qf[i][1]);
            const unsigned w0 = odd ? f[0] : f[2], w1 = odd ? f[1] : f[3];
            const float m = d - (bf_lo(w0) + bf_hi(w0) + bf_lo(w1));
            const float mh = bfround(m), mm = bfround(m - mh), ml = bfround(m - mh - mm);
            const unsigned n0 = cvtpk(-mh, -mm), n1 = cvtpk(-ml, 0.f);
            if (pad_here) {
                if (odd) { f[0] = n0; f[1] = n1; qf[i][0] = __builtin_bit_cast(bf16x8, f); }
                else { f[2] = n0; f[3] = n1; qf[i][1] = __builtin_bit_cast(bf16x8, f); }
            }
        }
    }
}

DI void ffn2_body(const u16* __restrict__ w2, char* ws, char* lds, int rt, int ct, int fw) {
    const int lane = threadIdx.x & 63, hh = lane >> 5;
    const unsigned ring = (unsigned)(uintptr_t)lds + K2_R + fw * 16384;
    const u16* wsrc = w2 + (size_t)(448 * fw + (lane >> 2)) * DM + 56 * ct + (lane & 3) * 8;
    const char* hsrc = ws + WS_H + (((size_t)(rt * 2) * 112 + 28 * fw) * 64 + lane) * 16;
    auto dma = [&](int ks) {
        const unsigned sl = RFL(ring + (ks & 3) * 4096);
        glds16(wsrc + (size_t)ks * 16 * DM, sl);
        glds16(wsrc + (size_t)ks * 16 * DM + 24, sl + 1024);
        glds16(hsrc + ks * 1024, sl + 2048);
        glds16(hsrc + ks * 1024 + 112 * 1024, sl + 3072);
    };
    dma(0); dma(1); dma(2);
    const int q4 = (lane & 15) >> 2, p4 = lane & 3, blk = (lane >> 4) & 1;
    lds_cptr wb = (lds_cptr)lds + K2_R + fw * 16384 + (4 * hh + q4) * 64 + blk * 32 + p4 * 8;
    lds_cptr hb = (lds_cptr)lds + K2_R + fw * 16384 + 2048 + lane * 16;
    f32x16 c[4];
#pragma unroll
    for (int i = 0; i < 4; ++i) c[i] = f32x16{};
#pragma unroll 1
    for (int k4 = 0; k4 < 7; ++k4) {
#pragma unroll
        for (int u = 0; u < 4; ++u) {
            const int ks = k4 * 4 + u;
            if (ks + 3 < 28) { dma(ks + 3); wait_vm<12>(); }
            else if (u == 1) wait_vm<8>();
            else if (u == 2) wait_vm<4>();
            else wait_vm<0>();
            const bf16x8 a0 = trfrag(wb + u * 4096, wb + u * 4096 + 512), a1 = trfrag(wb + u * 4096 + 1024, wb + u * 4096 + 1536);
            const bf16x8 b0 = lds128(hb + u * 4096), b1 = lds128(hb + u * 4096 + 1024);
            c[0] = MFMA(a0, b0, c[0]); c[1] = MFMA(a1, b0, c[1]); c[2] = MFMA(a0, b1, c[2]); c[3] = MFMA(a1, b1, c[3]);
            asm volatile("s_waitcnt lgkmcnt(0)" ::: "memory");
        }
    }
    LAS f32x4* pb = (LAS f32x4*)((lds_ptr)lds + K2_R) + lane;
#pragma unroll
    for (int i = 0; i < 4; ++i)
        if (i != fw) {
#pragma unroll
            for (int g = 0; g < 4; ++g) pb[fw * 1024 + i * 256 + g * 64] = f32x4{c[i][4 * g], c[i][4 * g + 1], c[i][4 * g + 2], c[i][4 * g + 3]};
        }
    bar_lds();
    f32x16 acc = fw == 0 ? c[0] : fw == 1 ? c[1] : fw == 2 ? c[2] : c[3];
#pragma unroll
    for (int p = 1; p < 4; ++p) {
        const int src = (fw + p) & 3;
#pragma unroll
        for (int g = 0; g < 4; ++g) {
            const f32x4 v = pb[src * 1024 + fw * 256 + g * 64];
#pragma unroll
            for (int e = 0; e < 4; ++e) acc[4 * g + e] += v[e];
        }
    }
    f32x4* bd = (f32x4*)(ws + WS_B) + ((size_t)(rt * 8 + ct) * 4 + fw) * 256 + lane;
#pragma unroll
    for (int g = 0; g < 4; ++g) bd[g * 64] = f32x4{acc[4 * g], acc[4 * g + 1], acc[4 * g + 2], acc[4 * g + 3]};
}

__global__ __launch_bounds__(768, 3) void k2_attn(const u16* __restrict__ w2, char* ws) {
    extern __shared__ __attribute__((aligned(16))) char lds[];
    const int xcd = blockIdx.x & 7, idx = blockIdx.x >> 3;
    const int w = RFL(threadIdx.x >> 6);
    if (w < 8) attn_body(ws, lds, 4 * (xcd >> 1) + (idx >> 3), 8 * (xcd & 1) + (idx & 7), w);
    else ffn2_body(w2, ws, lds, 4 * xcd + (idx & 3), idx >> 2, w - 8);
}

constexpr int K3_W = 0, K3_P = 57344, K3_LDS = K3_P + 16384;

__global__ __launch_bounds__(512, 2) void k3_out(const u16* __restrict__ wo, const char* __restrict__ ws, u16* __restrict__ out) {
    extern __shared__ __attribute__((aligned(16))) char lds[];
    const int xcd = blockIdx.x & 7, idx = blockIdx.x >> 3, rt = 4 * xcd + (idx & 3), ct = idx >> 2;
    const int w = RFL(threadIdx.x >> 6), lane = threadIdx.x & 63, r = lane & 31, hh = lane >> 5;
    const int tile = w & 3, kh = w >> 2, cb = tile & 1, rbk = tile >> 1;
    const unsigned l0 = (unsigned)(uintptr_t)lds;
#pragma unroll
    for (int i = 0; i < 7; ++i) {
        const int pc = w * 7 + i, pcb = pc / 28, prow = (pc % 28) * 16;
        glds16(wo + (size_t)(prow + (lane >> 2)) * DM + 56 * ct + pcb * 24 + (lane & 3) * 8, RFL(l0 + K3_W + pc * 1024));
    }
    const bf16x8* osrc = (const bf16x8*)(ws + WS_O) + ((size_t)(rt * 2 + rbk) * 32 + kh * 16) * 64 + lane;
    bf16x8 of[16];
#pragma unroll
    for (int i = 0; i < 16; ++i) of[i] = osrc[i * 64];
    f32x4 bp[4]; float g = 0.f;
    if (kh == 0) {
        const f32x4* bs = (const f32x4*)(ws + WS_B) + ((size_t)(rt * 8 + ct) * 4 + tile) * 256 + lane;
#pragma unroll
        for (int i = 0; i < 4; ++i) bp[i] = bs[i * 64];
        g = ((const float*)(ws + WS_G))[rt * 64 + rbk * 32 + r];
    }
    wait_bar<0>();
    const int q4 = (lane & 15) >> 2, p4 = lane & 3, blk = (lane >> 4) & 1;
    lds_cptr wb = (lds_cptr)lds + K3_W + cb * 28672 + kh * 8 * DH * 64 + (4 * hh + q4) * 64 + blk * 32 + p4 * 8;
    f32x16 acc = {};
#pragma unroll
    for (int i = 0; i < 16; ++i) {
        const int row0 = (((i >> 1) * DH) & ~7) + 16 * (i & 1);
        acc = MFMA(trfrag(wb + row0 * 64, wb + row0 * 64 + 512), of[i], acc);
    }
    LAS f32x4* pb = (LAS f32x4*)((lds_ptr)lds + K3_P) + tile * 256 + lane;
    if (kh == 1) {
#pragma unroll
        for (int gq = 0; gq < 4; ++gq) pb[gq * 64] = f32x4{acc[4 * gq], acc[4 * gq + 1], acc[4 * gq + 2], acc[4 * gq + 3]};
    }
    bar_lds();
    if (kh == 0) {
        u16* od = out + (size_t)(rt * 64 + rbk * 32 + r) * DM + 56 * ct + cb * 24 + 4 * hh;
#pragma unroll
        for (int gq = 0; gq < 4; ++gq) {
            const f32x4 v = pb[gq * 64];
            float y[4];
#pragma unroll
            for (int e = 0; e < 4; ++e) y[e] = g * (acc[4 * gq + e] + v[e]) + (1.f - g) * bp[gq][e];
            if (cb == 0 || gq > 0) *(u32x2*)(od + 8 * gq) = u32x2{cvtpk(y[0], y[1]), cvtpk(y[2], y[3])};
        }
    }
}

extern "C" void kernel_launch(void* const* d_in, const int* in_sizes, int n_in, void* d_out, int out_size, void* d_ws, size_t ws_size,
                              hipStream_t stream) {
    const u16* x = (const u16*)d_in[0];
    const u16* wqkv = (const u16*)d_in[1];
    const u16* wo = (const u16*)d_in[2];
    const u16* w1 = (const u16*)d_in[4];
    const u16* w2 = (const u16*)d_in[5];
    const u16* gw = (const u16*)d_in[6];
    char* ws = (char*)d_ws;
    (void)hipFuncSetAttribute((const void*)k1_proj, hipFuncAttributeMaxDynamicSharedMemorySize, K1_LDS);
    (void)hipFuncSetAttribute((const void*)k2_attn, hipFuncAttributeMaxDynamicSharedMemorySize, K2_LDS);
    (void)hipFuncSetAttribute((const void*)k3_out, hipFuncAttributeMaxDynamicSharedMemorySize, K3_LDS);
    k1_proj<<<256, 512, K1_LDS, stream>>>(x, wqkv, w1, gw, ws);
    k2_attn<<<256, 768, K2_LDS, stream>>>(w2, ws);
    k3_out<<<256, 512, K3_LDS, stream>>>(wo, ws, (u16*)d_out);
}
```

```cpp
#include <hip/hip_runtime.h>
#include <hip/hip_bf16.h>
#include <stdint.h>

typedef __attribute__((ext_vector_type(8))) short bf16x8;
typedef __attribute__((ext_vector_type(4))) short s16x4;
typedef __attribute__((ext_vector_type(16))) float f32x16;
typedef __attribute__((ext_vector_type(4))) float f32x4;
typedef __attribute__((ext_vector_type(4))) unsigned u32x4;
typedef __attribute__((ext_vector_type(2))) unsigned u32x2;
typedef __attribute__((ext_vector_type(16))) unsigned u32x16;
typedef unsigned short u16;
typedef unsigned long long u64;
#define LAS __attribute__((address_space(3)))
#define GAS __attribute__((address_space(1)))
typedef LAS const char* lds_cptr;
typedef LAS char* lds_ptr;
typedef GAS u64 gu64;
typedef GAS unsigned gu32;
#define DI __device__ __forceinline__
#define MFMA(a, b, c) __builtin_amdgcn_mfma_f32_32x32x16_bf16((a), (b), (c), 0, 0, 0)
#define RLX_AGENT __ATOMIC_RELAXED, __HIP_MEMORY_SCOPE_AGENT
#define SBAR() __builtin_amdgcn_sched_barrier(0)
#define PIN(x) asm volatile("" : "+v"(x))
#define RFL(x) __builtin_amdgcn_readfirstlane(x)

constexpr int DM = 448, DH = 28, FF = 1792, NQKV = 1344;
constexpr float LOG2E = 1.4426950408889634f;
constexpr unsigned WS_EP = 0;
constexpr unsigned FL = 256;
constexpr unsigned WS_G = 4096;
constexpr unsigned WS_FKV = 65536;
constexpr unsigned WS_FH = WS_FKV + 256 * FL;
constexpr unsigned WS_FO = WS_FH + 512 * FL;
constexpr unsigned KVROW = 57344;
constexpr unsigned WS_KV = 1u << 20;
constexpr unsigned WS_H = WS_KV + (4u << 20);
constexpr unsigned WS_O = WS_H + (8u << 20);
constexpr unsigned WS_END = WS_O + (2u << 20);

constexpr int L_RING = 114688, L_MISC = 155648, L_TOTAL = 163840;
constexpr int L_CTL = L_MISC + 4096;
DI constexpr int kv_off(int c) { return L_RING + ((c + 4) % 5) * 8192; }
DI constexpr int wq_off(int c) { return c == 0 ? L_MISC : L_RING + 8192 + (c - 1) * 4096; }
constexpr int L_Q = L_RING + 4 * 8192, L_MERGE = 0, L_MX = L_MISC;
constexpr int L_EPOCH = L_CTL + 64, L_SINK = L_CTL + 128, L_GATE = L_SINK + 256, L_ID = L_CTL + 2048;
enum { C_W1A = 0, C_Q = 1, C_FFN1 = 2, C_WQ = 3, C_MFREE = 4, C_W1B = 5, C_KVD = 6, C_M1 = 7, C_M2 = 8, C_MX = 9, C_X6 = 10 };
DI constexpr int w2_blk(int k) { return k < 7 ? k : k < 11 ? k + 7 : k < 22 ? k - 4 : k < 33 ? k - 15 : k - 26; }

DI int lane_id() { int l; asm volatile("v_mbcnt_lo_u32_b32 %0, -1, 0\n\tv_mbcnt_hi_u32_b32 %0, -1, %0" : "=v"(l)); return l; }
DI unsigned cvtpk(float lo, float hi) {
    typedef __attribute__((ext_vector_type(2))) float f2; typedef __attribute__((ext_vector_type(2))) __bf16 b2;
    f2 v = {lo, hi}; b2 r = __builtin_convertvector(v, b2); return __builtin_bit_cast(unsigned, r);
}
DI void glds16s(unsigned voff, const void* sbase, unsigned lds_base) {
    unsigned sv;
    asm volatile("s_mov_b32 %0, m0\n\ts_mov_b32 m0, %3\n\ts_nop 0\n\tglobal_load_lds_dwordx4 %1, %2\n\ts_mov_b32 m0, %0"
                 : "=&s"(sv) : "v"(voff), "s"(sbase), "s"(lds_base) : "memory");
}
DI void touch(const void* g, unsigned lds_sink) {
    unsigned sv;
    asm volatile("s_mov_b32 %0, m0\n\ts_mov_b32 m0, %2\n\ts_nop 0\n\tglobal_load_lds_dword %1, off\n\ts_mov_b32 m0, %0"
                 : "=&s"(sv) : "v"(g), "s"(lds_sink) : "memory");
}
DI void touch32(unsigned voff, const void* sbase, unsigned lds_sink) {
    unsigned sv;
    asm volatile("s_mov_b32 %0, m0\n\ts_mov_b32 m0, %3\n\ts_mov_b32 exec_hi, 0\n\tglobal_load_lds_dword %1, %2\n\ts_mov_b32 exec_hi, -1\n\ts_mov_b32 m0, %0"
                 : "=&s"(sv) : "v"(voff), "s"(sbase), "s"(lds_sink) : "memory");
}
DI void glds4_dev(const void* g, unsigned lds_sink) {
    unsigned sv;
    asm volatile("s_mov_b32 %0, m0\n\ts_mov_b32 m0, %2\n\ts_nop 0\n\tglobal_load_lds_dword %1, off sc1\n\ts_mov_b32 m0, %0"
                 : "=&s"(sv) : "v"(g), "s"(lds_sink) : "memory");
}
DI bf16x8 lds128(lds_cptr p) { return *(const LAS bf16x8*)p; }
DI s16x4 ldstr(lds_cptr p) {
    typedef short v4i16_t __attribute__((ext_vector_type(4)));
    return __builtin_bit_cast(s16x4, __builtin_amdgcn_ds_read_tr16_b64_v4i16((LAS v4i16_t*)p));
}
DI bf16x8 trfrag(lds_cptr p0, lds_cptr p1) { s16x4 a = ldstr(p0), b = ldstr(p1); return __builtin_shufflevector(a, b, 0, 1, 2, 3, 4, 5, 6, 7); }
DI u32x4 pack8u(const f32x16& x, int s) {
    return u32x4{cvtpk(x[8 * s], x[8 * s + 1]), cvtpk(x[8 * s + 2], x[8 * s + 3]), cvtpk(x[8 * s + 4], x[8 * s + 5]), cvtpk(x[8 * s + 6], x[8 * s + 7])};
}
DI bf16x8 pack8(const f32x16& x, int s) { return __builtin_bit_cast(bf16x8, pack8u(x, s)); }
DI float swap32(float v) {
    auto r = __builtin_amdgcn_permlane32_swap(__float_as_uint(v), __float_as_uint(v), false, false);
    return (lane_id() & 32) ? __uint_as_float(r[0]) : __uint_as_float(r[1]);
}
DI float sigmoidf_(float z) { return __builtin_amdgcn_rcpf(1.f + __builtin_amdgcn_exp2f(-LOG2E * z)); }
template <int N> DI void wait_bar() { asm volatile("s_waitcnt vmcnt(%0) lgkmcnt(0)\n\ts_barrier" ::"n"(N) : "memory"); }
template <int N> DI void wait_vm() { asm volatile("s_waitcnt vmcnt(%0)" ::"n"(N) : "memory"); }
DI void bar_lds() { asm volatile("s_waitcnt lgkmcnt(0)\n\ts_barrier" ::: "memory"); }

typedef __amdgpu_buffer_rsrc_t rsrc_t;
DI void st16(rsrc_t rs, u32x4 v, unsigned voff, unsigned soff) { __builtin_amdgcn_raw_buffer_store_b128(v, rs, voff, soff, 16); }
DI bf16x8 ld16(rsrc_t rs, unsigned voff, unsigned soff) { return __builtin_bit_cast(bf16x8, __builtin_amdgcn_raw_buffer_load_b128(rs, voff, soff, 16)); }
DI void publish(char* ws, unsigned flag_off, u64 tag) {
    asm volatile("s_waitcnt vmcnt(0)" ::: "memory");
    if (lane_id() == 0) __hip_atomic_store((gu64*)(ws + flag_off), tag, RLX_AGENT);
}
DI void poll_at(char* ws, unsigned lane_off, int n, u64 tag) {
    gu64* p = (gu64*)(ws + lane_off);
    if (lane_id() < n) {
#pragma unroll 1
        for (unsigned spins = 0; spins < (1u << 18); ++spins) {
            const u64 v = __hip_atomic_load(p, RLX_AGENT);
            if (__all(v == tag)) break;
            __builtin_amdgcn_s_sleep(1);
        }
    }
    __builtin_amdgcn_fence(__ATOMIC_ACQUIRE, "wavefront");
    asm volatile("" ::: "memory");
}
DI void lds_arrive(char* lds, int word, unsigned v = 1u) {
    u64 sv;
    asm volatile("s_mov_b64 %0, exec\n\ts_mov_b64 exec, 1\n\tds_add_u32 %1, %2\n\ts_mov_b64 exec, %0"
                 : "=&s"(sv) : "v"((unsigned)(uintptr_t)lds + L_CTL + word * 4), "v"(v) : "memory");
}
template <int SLEEP = 1> DI unsigned lds_wait(char* lds, int word, unsigned n) {
    unsigned v;
    while (((v = RFL(__hip_atomic_load((LAS unsigned*)((lds_ptr)lds + L_CTL) + word, __ATOMIC_RELAXED, __HIP_MEMORY_SCOPE_WORKGROUP))) & 0xffffu) < n)
        if (SLEEP) __builtin_amdgcn_s_sleep(SLEEP);
    asm volatile("" ::: "memory");
    return v;
}

DI u64 make_tag(char* lds) {
    const u32x2 ev = *(const volatile LAS u32x2*)((lds_cptr)lds + L_EPOCH);
    u64 tag = (((u64)(unsigned)RFL(ev[1]) << 32) | (unsigned)RFL(ev[0])) + 0x9E3779B97F4A7C15ull;
    tag = (tag ^ (tag >> 30)) * 0xBF58476D1CE4E5B9ull; tag = (tag ^ (tag >> 27)) * 0x94D049BB133111EBull;
    return tag ^ (tag >> 31);
}

struct Ptrs { const u16 *x, *wqkv, *wo, *w1, *w2, *gw; u16* out; char* ws;
};

DI u64 gate_word(float g, u64 tag) { return (tag << 24) | (__float_as_uint(g) >> 8); }
DI u64 gate_load(char* ws, int row) { return __hip_atomic_load((gu64*)(ws + WS_G) + row, RLX_AGENT); }
DI float gate_get(char* ws, int row, u64 v, u64 tag) {
#pragma unroll 1
    for (unsigned spins = 0; spins < (1u << 18) && !__all((v >> 24) == (tag & 0xffffffffffull)); ++spins) { __builtin_amdgcn_s_sleep(1); v = gate_load(ws, row); }
    return __uint_as_float((unsigned)v << 8);
}

DI int w_lane_off(int lane, bool nat) {
    const int hh = lane >> 5, q4 = (lane & 15) >> 2, p4 = lane & 3, blk = (lane >> 4) & 1;
    const int pp = nat ? (((p4 & 1) << 1) | (p4 >> 1)) : p4;
    return (8 * hh + q4) * 128 + (((blk * 2 + (pp >> 1)) ^ ((q4 >> 1) << 2)) << 4) + (pp & 1) * 8;
}
DI void x_bases(lds_cptr (&xb)[4], char* lds, int rb, int lane) {
    const int r = lane & 31, hh = lane >> 5, sw = (r >> 1) & 7;
#pragma unroll
    for (int j = 0; j < 4; ++j) xb[j] = (lds_cptr)lds + (32 * rb + r) * 128 + (((2 * j + hh) ^ sw) << 4);
}

DI u32x16 cat4(bf16x8 a, bf16x8 b, bf16x8 c, bf16x8 d) {
    const u32x4 w = __builtin_bit_cast(u32x4, a), x = __builtin_bit_cast(u32x4, b), y = __builtin_bit_cast(u32x4, c), z = __builtin_bit_cast(u32x4, d);
    return u32x16{w[0], w[1], w[2], w[3], x[0], x[1], x[2], x[3], y[0], y[1], y[2], y[3], z[0], z[1], z[2], z[3]};
}
DI bf16x8 part4(const u32x16& v, int i) { return __builtin_bit_cast(bf16x8, u32x4{v[4 * i], v[4 * i + 1], v[4 * i + 2], v[4 * i + 3]}); }
template <bool ISV>
DI void kv_part(u32x16 (&xv)[7], rsrc_t rs, char* lds, int j, int t, int rb) {
    f32x16 c0 = {}, c1 = {};
    {
        const int lane = lane_id();
        lds_cptr xb[4]; x_bases(xb, lds, rb, lane);
        const int wo = w_lane_off(lane, !ISV);
        lds_cptr w0 = (lds_cptr)lds + L_RING + wo, w1 = (lds_cptr)lds + L_RING + (wo ^ 64);
        bar_lds();
#pragma unroll
        for (int s = 0; s < 7; ++s) {
            const int o = kv_off(s) - L_RING;
            bf16x8 wa[2], wb[2];
            auto rdw = [&](int u) { wa[u & 1] = trfrag(w0 + o + u * 2048, w0 + o + u * 2048 + 512); wb[u & 1] = trfrag(w1 + o + u * 2048, w1 + o + u * 2048 + 512); };
            const bf16x8 x0 = lds128(xb[0] + s * 16384); rdw(0);
            const bf16x8 x1 = lds128(xb[1] + s * 16384); rdw(1);
            xv[s] = cat4(x0, x1, lds128(xb[2] + s * 16384), lds128(xb[3] + s * 16384));
            SBAR();
#pragma unroll
            for (int u = 0; u < 4; ++u) {
                if (u == 3 && s < 6) bar_lds();
                const bf16x8 xf = part4(xv[s], u);
                if (ISV) { c0 = MFMA(xf, wa[u & 1], c0); c1 = MFMA(xf, wb[u & 1], c1); }
                else { c0 = MFMA(wa[u & 1], xf, c0); c1 = MFMA(wb[u & 1], xf, c1); }
                PIN(c0); PIN(c1); SBAR();
                if (u < 2) { rdw(u + 2); SBAR(); }
                if (u == 1 && s == 6) lds_arrive(lds, C_X6);
            }
        }
    }
    const int lane = lane_id(), r = lane & 31, hh = lane >> 5;
    const unsigned base = WS_KV + (unsigned)(t * 4 + rb) * KVROW + (ISV ? 28672u + (j - 7) * 4096u : j * 4096u);
    const unsigned vo = ISV ? (r >> 3) * 512 + hh * 128 + (r & 7) * 16 : lane * 16;
    st16(rs, pack8u(c0, 0), vo, base); st16(rs, pack8u(c0, 1), vo, base + (ISV ? 256 : 1024));
    st16(rs, pack8u(c1, 0), vo, base + 2048); st16(rs, pack8u(c1, 1), vo, base + 2048 + (ISV ? 256 : 1024));
}

DI void x_only(u32x16 (&xv)[7], char* lds, int rb) {
    lds_cptr xb[4]; x_bases(xb, lds, rb, lane_id());
#pragma unroll
    for (int s = 0; s < 7; ++s) {
        bar_lds();
        xv[s] = cat4(lds128(xb[0] + s * 16384), lds128(xb[1] + s * 16384), lds128(xb[2] + s * 16384), lds128(xb[3] + s * 16384));
    }
}
DI u32x4 srd(const void* p, unsigned bytes) { const u64 a = (u64)(uintptr_t)p; return u32x4{(unsigned)a, (unsigned)(a >> 32) & 0xffffu, bytes, 0x00020000u}; }
DI void ffn_up(const u32x16 (&xv)[7], unsigned lo, unsigned vo, u32x4 rs, unsigned so) {
    asm volatile(
    "s_setprio 2\n\t"
    "s_nop 4\n\t"
    "ds_read_b64_tr_b16 v[144:145], %[lo] offset:0\n\t"
    "ds_read_b64_tr_b16 v[146:147], %[lo] offset:512\n\t"
    "ds_read_b64_tr_b16 v[148:149], %[lo] offset:2048\n\t"
    "ds_read_b64_tr_b16 v[150:151], %[lo] offset:2560\n\t"
    "ds_read_b64_tr_b16 v[152:153], %[lo] offset:4096\n\t"
    "ds_read_b64_tr_b16 v[154:155], %[lo] offset:4608\n\t"
    "ds_read_b64_tr_b16 v[156:157], %[lo] offset:6144\n\t"
    "ds_read_b64_tr_b16 v[158:159], %[lo] offset:6656\n\t"
    "s_waitcnt lgkmcnt(4)\n\t"
    "v_mfma_f32_32x32x16_bf16 v[112:127], v[144:147], v[0:3], 0\n\t"
    "ds_read_b64_tr_b16 v[144:145], %[lo] offset:8192\n\t"
    "ds_read_b64_tr_b16 v[146:147], %[lo] offset:8704\n\t"
    "v_mfma_f32_32x32x16_bf16 v[112:127], v[148:151], v[4:7], v[112:127]\n\t"
    "ds_read_b64_tr_b16 v[148:149], %[lo] offset:10240\n\t"
    "ds_read_b64_tr_b16 v[150:151], %[lo] offset:10752\n\t"
    "s_waitcnt lgkmcnt(4)\n\t"
    "v_mfma_f32_32x32x16_bf16 v[112:127], v[152:155], v[8:11], v[112:127]\n\t"
    "ds_read_b64_tr_b16 v[152:153], %[lo] offset:12288\n\t"
    "ds_read_b64_tr_b16 v[154:155], %[lo] offset:12800\n\t"
    "v_mfma_f32_32x32x16_bf16 v[112:127], v[156:159], v[12:15], v[112:127]\n\t"
    "ds_read_b64_tr_b16 v[156:157], %[lo] offset:14336\n\t"
    "ds_read_b64_tr_b16 v[158:159], %[lo] offset:14848\n\t"
    "s_setprio 3\n\t"
    "s_waitcnt lgkmcnt(4)\n\t"
    "v_mfma_f32_32x32x16_bf16 v[112:127], v[144:147], v[16:19], v[112:127]\n\t"
    "ds_read_b64_tr_b16 v[144:145], %[lo] offset:16384\n\t"
    "ds_read_b64_tr_b16 v[146:147], %[lo] offset:16896\n\t"
    "v_mfma_f32_32x32x16_bf16 v[112:127], v[148:151], v[20:23], v[112:127]\n\t"
    "ds_read_b64_tr_b16 v[148:149], %[lo] offset:18432\n\t"
    "ds_read_b64_tr_b16 v[150:151], %[lo] offset:18944\n\t"
    "s_waitcnt lgkmcnt(4)\n\t"
    "v_mfma_f32_32x32x16_bf16 v[112:127], v[152:155], v[24:27], v[112:127]\n\t"
    "ds_read_b64_tr_b16 v[152:153], %[lo] offset:20480\n\t"
    "ds_read_b64_tr_b16 v[154:155], %[lo] offset:20992\n\t"
    "v_mfma_f32_32x32x16_bf16 v[112:127], v[156:159], v[28:31], v[112:127]\n\t"
    "ds_read_b64_tr_b16 v[156:157], %[lo] offset:22528\n\t"
    "ds_read_b64_tr_b16 v[158:159], %[lo] offset:23040\n\t"
    "s_waitcnt lgkmcnt(4)\n\t"
    "v_mfma_f32_32x32x16_bf16 v[112:127], v[144:147], v[32:35], v[112:127]\n\t"
    "ds_read_b64_tr_b16 v[144:145], %[lo] offset:24576\n\t"
    "ds_read_b64_tr_b16 v[146:147], %[lo] offset:25088\n\t"
    "v_mfma_f32_32x32x16_bf16 v[112:127], v[148:151], v[36:39], v[112:127]\n\t"
    "ds_read_b64_tr_b16 v[148:149], %[lo] offset:26624\n\t"
    "ds_read_b64_tr_b16 v[150:151], %[lo] offset:27136\n\t"
    "s_waitcnt lgkmcnt(4)\n\t"
    "v_mfma_f32_32x32x16_bf16 v[112:127], v[152:155], v[40:43], v[112:127]\n\t"
    "ds_read_b64_tr_b16 v[152:153], %[lo] offset:28672\n\t"
    "ds_read_b64_tr_b16 v[154:155], %[lo] offset:29184\n\t"
    "v_mfma_f32_32x32x16_bf16 v[112:127], v[156:159], v[44:47], v[112:127]\n\t"
    "ds_read_b64_tr_b16 v[156:157], %[lo] offset:30720\n\t"
    "ds_read_b64_tr_b16 v[158:159], %[lo] offset:31232\n\t"
    "s_waitcnt lgkmcnt(4)\n\t"
    "v_mfma_f32_32x32x16_bf16 v[112:127], v[144:147], v[48:51], v[112:127]\n\t"
    "ds_read_b64_tr_b16 v[144:145], %[lo] offset:32768\n\t"
    "ds_read_b64_tr_b16 v[146:147], %[lo] offset:33280\n\t"
    "v_mfma_f32_32x32x16_bf16 v[112:127], v[148:151], v[52:55], v[112:127]\n\t"
    "ds_read_b64_tr_b16 v[148:149], %[lo] offset:34816\n\t"
    "ds_read_b64_tr_b16 v[150:151], %[lo] offset:35328\n\t"
    "s_waitcnt lgkmcnt(4)\n\t"
    "v_mfma_f32_32x32x16_bf16 v[112:127], v[152:155], v[56:59], v[112:127]\n\t"
    "ds_read_b64_tr_b16 v[152:153], %[lo] offset:36864\n\t"
    "ds_read_b64_tr_b16 v[154:155], %[lo] offset:37376\n\t"
    "v_mfma_f32_32x32x16_bf16 v[112:127], v[156:159], v[60:63], v[112:127]\n\t"
    "ds_read_b64_tr_b16 v[156:157], %[lo] offset:38912\n\t"
    "ds_read_b64_tr_b16 v[158:159], %[lo] offset:39424\n\t"
    "s_waitcnt lgkmcnt(4)\n\t"
    "v_mfma_f32_32x32x16_bf16 v[112:127], v[144:147], v[64:67], v[112:127]\n\t"
    "ds_read_b64_tr_b16 v[144:145], %[lo] offset:40960\n\t"
    "ds_read_b64_tr_b16 v[146:147], %[lo] offset:41472\n\t"
    "v_mfma_f32_32x32x16_bf16 v[112:127], v[148:151], v[68:71], v[112:127]\n\t"
    "ds_read_b64_tr_b16 v[148:149], %[lo] offset:43008\n\t"
    "ds_read_b64_tr_b16 v[150:151], %[lo] offset:43520\n\t"
    "s_waitcnt lgkmcnt(4)\n\t"
    "v_mfma_f32_32x32x16_bf16 v[112:127], v[152:155], v[72:75], v[112:127]\n\t"
    "ds_read_b64_tr_b16 v[152:153], %[lo] offset:45056\n\t"
    "ds_read_b64_tr_b16 v[154:155], %[lo] offset:45568\n\t"
    "v_mfma_f32_32x32x16_bf16 v[112:127], v[156:159], v[76:79], v[112:127]\n\t"
    "ds_read_b64_tr_b16 v[156:157], %[lo] offset:47104\n\t"
    "ds_read_b64_tr_b16 v[158:159], %[lo] offset:47616\n\t"
    "s_waitcnt lgkmcnt(4)\n\t"
    "v_mfma_f32_32x32x16_bf16 v[112:127], v[144:147], v[80:83], v[112:127]\n\t"
    "ds_read_b64_tr_b16 v[144:145], %[lo] offset:49152\n\t"
    "ds_read_b64_tr_b16 v[146:147], %[lo] offset:49664\n\t"
    "v_mfma_f32_32x32x16_bf16 v[112:127], v[148:151], v[84:87], v[112:127]\n\t"
    "ds_read_b64_tr_b16 v[148:149], %[lo] offset:51200\n\t"
    "ds_read_b64_tr_b16 v[150:151], %[lo] offset:51712\n\t"
    "s_waitcnt lgkmcnt(4)\n\t"
    "v_mfma_f32_32x32x16_bf16 v[112:127], v[152:155], v[88:91], v[112:127]\n\t"
    "ds_read_b64_tr_b16 v[152:153], %[lo] offset:53248\n\t"
    "ds_read_b64_tr_b16 v[154:155], %[lo] offset:53760\n\t"
    "v_mfma_f32_32x32x16_bf16 v[112:127], v[156:159], v[92:95], v[112:127]\n\t"
    "ds_read_b64_tr_b16 v[156:157], %[lo] offset:55296\n\t"
    "ds_read_b64_tr_b16 v[158:159], %[lo] offset:55808\n\t"
    "s_waitcnt lgkmcnt(4)\n\t"
    "v_mfma_f32_32x32x16_bf16 v[112:127], v[144:147], v[96:99], v[112:127]\n\t"
    "v_xor_b32 %[lo], 64, %[lo]\n\t"
    "ds_read_b64_tr_b16 v[144:145], %[lo] offset:0\n\t"
    "ds_read_b64_tr_b16 v[146:147], %[lo] offset:512\n\t"
    "v_mfma_f32_32x32x16_bf16 v[112:127], v[148:151], v[100:103], v[112:127]\n\t"
    "ds_read_b64_tr_b16 v[148:149], %[lo] offset:2048\n\t"
    "ds_read_b64_tr_b16 v[150:151], %[lo] offset:2560\n\t"
    "s_waitcnt lgkmcnt(4)\n\t"
    "v_mfma_f32_32x32x16_bf16 v[112:127], v[152:155], v[104:107], v[112:127]\n\t"
    "ds_read_b64_tr_b16 v[152:153], %[lo] offset:4096\n\t"
    "ds_read_b64_tr_b16 v[154:155], %[lo] offset:4608\n\t"
    "v_mfma_f32_32x32x16_bf16 v[112:127], v[156:159], v[108:111], v[112:127]\n\t"
    "ds_read_b64_tr_b16 v[156:157], %[lo] offset:6144\n\t"
    "ds_read_b64_tr_b16 v[158:159], %[lo] offset:6656\n\t"
    "s_setprio 1\n\t"
    "s_nop 11\n\t"
    "v_mul_f32 v160, 0xbfb8aa3b, v112\n\t"
    "v_mul_f32 v161, 0xbfb8aa3b, v113\n\t"
    "v_mul_f32 v162, 0xbfb8aa3b, v114\n\t"
    "v_mul_f32 v163, 0xbfb8aa3b, v115\n\t"
    "v_exp_f32 v160, v160\n\t"
    "v_exp_f32 v161, v161\n\t"
    "v_exp_f32 v162, v162\n\t"
    "v_exp_f32 v163, v163\n\t"
    "v_add_f32 v160, 1.0, v160\n\t"
    "v_add_f32 v161, 1.0, v161\n\t"
    "v_add_f32 v162, 1.0, v162\n\t"
    "v_add_f32 v163, 1.0, v163\n\t"
    "v_rcp_f32 v160, v160\n\t"
    "v_rcp_f32 v161, v161\n\t"
    "v_rcp_f32 v162, v162\n\t"
    "v_rcp_f32 v163, v163\n\t"
    "v_mul_f32 v160, v112, v160\n\t"
    "v_mul_f32 v161, v113, v161\n\t"
    "v_mul_f32 v162, v114, v162\n\t"
    "v_mul_f32 v163, v115, v163\n\t"
    "v_cvt_pk_bf16_f32 v112, v160, v161\n\t"
    "v_cvt_pk_bf16_f32 v113, v162, v163\n\t"
    "v_mul_f32 v160, 0xbfb8aa3b, v116\n\t"
    "v_mul_f32 v161, 0xbfb8aa3b, v117\n\t"
    "v_mul_f32 v162, 0xbfb8aa3b, v118\n\t"
    "v_mul_f32 v163, 0xbfb8aa3b, v119\n\t"
    "v_exp_f32 v160, v160\n\t"
    "v_exp_f32 v161, v161\n\t"
    "v_exp_f32 v162, v162\n\t"
    "v_exp_f32 v163, v163\n\t"
    "v_add_f32 v160, 1.0, v160\n\t"
    "v_add_f32 v161, 1.0, v161\n\t"
    "v_add_f32 v162, 1.0, v162\n\t"
    "v_add_f32 v163, 1.0, v163\n\t"
    "v_rcp_f32 v160, v160\n\t"
    "v_rcp_f32 v161, v161\n\t"
    "v_rcp_f32 v162, v162\n\t"
    "v_rcp_f32 v163, v163\n\t"
    "v_mul_f32 v160, v116, v160\n\t"
    "v_mul_f32 v161, v117, v161\n\t"
    "v_mul_f32 v162, v118, v162\n\t"
    "v_mul_f32 v163, v119, v163\n\t"
    "v_cvt_pk_bf16_f32 v114, v160, v161\n\t"
    "v_cvt_pk_bf16_f32 v115, v162, v163\n\t"
    "buffer_store_dwordx4 v[112:115], %[vo], %[rs], %[so] offen offset:0 sc1\n\t"
    "v_mul_f32 v160, 0xbfb8aa3b, v120\n\t"
    "v_mul_f32 v161, 0xbfb8aa3b, v121\n\t"
    "v_mul_f32 v162, 0xbfb8aa3b, v122\n\t"
    "v_mul_f32 v163, 0xbfb8aa3b, v123\n\t"
    "v_exp_f32 v160, v160\n\t"
    "v_exp_f32 v161, v161\n\t"
    "v_exp_f32 v162, v162\n\t"
    "v_exp_f32 v163, v163\n\t"
    "v_add_f32 v160, 1.0, v160\n\t"
    "v_add_f32 v161, 1.0, v161\n\t"
    "v_add_f32 v162, 1.0, v162\n\t"
    "v_add_f32 v163, 1.0, v163\n\t"
    "v_rcp_f32 v160, v160\n\t"
    "v_rcp_f32 v161, v161\n\t"
    "v_rcp_f32 v162, v162\n\t"
    "v_rcp_f32 v163, v163\n\t"
    "v_mul_f32 v160, v120, v160\n\t"
    "v_mul_f32 v161, v121, v161\n\t"
    "v_mul_f32 v162, v122, v162\n\t"
    "v_mul_f32 v163, v123, v163\n\t"
    "v_cvt_pk_bf16_f32 v116, v160, v161\n\t"
    "v_cvt_pk_bf16_f32 v117, v162, v163\n\t"
    "v_mul_f32 v160, 0xbfb8aa3b, v124\n\t"
    "v_mul_f32 v161, 0xbfb8aa3b, v125\n\t"
    "v_mul_f32 v162, 0xbfb8aa3b, v126\n\t"
    "v_mul_f32 v163, 0xbfb8aa3b, v127\n\t"
    "v_exp_f32 v160, v160\n\t"
    "v_exp_f32 v161, v161\n\t"
    "v_exp_f32 v162, v162\n\t"
    "v_exp_f32 v163, v163\n\t"
    "v_add_f32 v160, 1.0, v160\n\t"
    "v_add_f32 v161, 1.0, v161\n\t"
    "v_add_f32 v162, 1.0, v162\n\t"
    "v_add_f32 v163, 1.0, v163\n\t"
    "v_rcp_f32 v160, v160\n\t"
    "v_rcp_f32 v161, v161\n\t"
    "v_rcp_f32 v162, v162\n\t"
    "v_rcp_f32 v163, v163\n\t"
    "v_mul_f32 v160, v124, v160\n\t"
    "v_mul_f32 v161, v125, v161\n\t"
    "v_mul_f32 v162, v126, v162\n\t"
    "v_mul_f32 v163, v127, v163\n\t"
    "v_cvt_pk_bf16_f32 v118, v160, v161\n\t"
    "v_cvt_pk_bf16_f32 v119, v162, v163\n\t"
    "buffer_store_dwordx4 v[116:119], %[vo], %[rs], %[so] offen offset:1024 sc1\n\t"
    "s_setprio 2\n\t"
    "s_waitcnt lgkmcnt(4)\n\t"
    "v_mfma_f32_32x32x16_bf16 v[128:143], v[144:147], v[0:3], 0\n\t"
    "ds_read_b64_tr_b16 v[144:145], %[lo] offset:8192\n\t"
    "ds_read_b64_tr_b16 v[146:147], %[lo] offset:8704\n\t"
    "v_mfma_f32_32x32x16_bf16 v[128:143], v[148:151], v[4:7], v[128:143]\n\t"
    "ds_read_b64_tr_b16 v[148:149], %[lo] offset:10240\n\t"
    "ds_read_b64_tr_b16 v[150:151], %[lo] offset:10752\n\t"
    "s_waitcnt lgkmcnt(4)\n\t"
    "v_mfma_f32_32x32x16_bf16 v[128:143], v[152:155], v[8:11], v[128:143]\n\t"
    "ds_read_b64_tr_b16 v[152:153], %[lo] offset:12288\n\t"
    "ds_read_b64_tr_b16 v[154:155], %[lo] offset:12800\n\t"
    "v_mfma_f32_32x32x16_bf16 v[128:143], v[156:159], v[12:15], v[128:143]\n\t"
    "ds_read_b64_tr_b16 v[156:157], %[lo] offset:14336\n\t"
    "ds_read_b64_tr_b16 v[158:159], %[lo] offset:14848\n\t"
    "s_setprio 3\n\t"
    "s_waitcnt lgkmcnt(4)\n\t"
    "v_mfma_f32_32x32x16_bf16 v[128:143], v[144:147], v[16:19], v[128:143]\n\t"
    "ds_read_b64_tr_b16 v[144:145], %[lo] offset:16384\n\t"
    "ds_read_b64_tr_b16 v[146:147], %[lo] offset:16896\n\t"
    "v_mfma_f32_32x32x16_bf16 v[128:143], v[148:151], v[20:23], v[128:143]\n\t"
    "ds_read_b64_tr_b16 v[148:149], %[lo] offset:18432\n\t"
    "ds_read_b64_tr_b16 v[150:151], %[lo] offset:18944\n\t"
    "s_waitcnt lgkmcnt(4)\n\t"
    "v_mfma_f32_32x32x16_bf16 v[128:143], v[152:155], v[24:27], v[128:143]\n\t"
    "ds_read_b64_tr_b16 v[152:153], %[lo] offset:20480\n\t"
    "ds_read_b64_tr_b16 v[154:155], %[lo] offset:20992\n\t"
    "v_mfma_f32_32x32x16_bf16 v[128:143], v[156:159], v[28:31], v[128:143]\n\t"
    "ds_read_b64_tr_b16 v[156:157], %[lo] offset:22528\n\t"
    "ds_read_b64_tr_b16 v[158:159], %[lo] offset:23040\n\t"
    "s_waitcnt lgkmcnt(4)\n\t"
    "v_mfma_f32_32x32x16_bf16 v[128:143], v[144:147], v[32:35], v[128:143]\n\t"
    "ds_read_b64_tr_b16 v[144:145], %[lo] offset:24576\n\t"
    "ds_read_b64_tr_b16 v[146:147], %[lo] offset:25088\n\t"
    "v_mfma_f32_32x32x16_bf16 v[128:143], v[148:151], v[36:39], v[128:143]\n\t"
    "ds_read_b64_tr_b16 v[148:149], %[lo] offset:26624\n\t"
    "ds_read_b64_tr_b16 v[150:151], %[lo] offset:27136\n\t"
    "s_waitcnt lgkmcnt(4)\n\t"
    "v_mfma_f32_32x32x16_bf16 v[128:143], v[152:155], v[40:43], v[128:143]\n\t"
    "ds_read_b64_tr_b16 v[152:153], %[lo] offset:28672\n\t"
    "ds_read_b64_tr_b16 v[154:155], %[lo] offset:29184\n\t"
    "v_mfma_f32_32x32x16_bf16 v[128:143], v[156:159], v[44:47], v[128:143]\n\t"
    "ds_read_b64_tr_b16 v[156:157], %[lo] offset:30720\n\t"
    "ds_read_b64_tr_b16 v[158:159], %[lo] offset:31232\n\t"
    "s_waitcnt lgkmcnt(4)\n\t"
    "v_mfma_f32_32x32x16_bf16 v[128:143], v[144:147], v[48:51], v[128:143]\n\t"
    "ds_read_b64_tr_b16 v[144:145], %[lo] offset:32768\n\t"
    "ds_read_b64_tr_b16 v[146:147], %[lo] offset:33280\n\t"
    "v_mfma_f32_32x32x16_bf16 v[128:143], v[148:151], v[52:55], v[128:143]\n\t"
    "ds_read_b64_tr_b16 v[148:149], %[lo] offset:34816\n\t"
    "ds_read_b64_tr_b16 v[150:151], %[lo] offset:35328\n\t"
    "s_waitcnt lgkmcnt(4)\n\t"
    "v_mfma_f32_32x32x16_bf16 v[128:143], v[152:155], v[56:59], v[128:143]\n\t"
    "ds_read_b64_tr_b16 v[152:153], %[lo] offset:36864\n\t"
    "ds_read_b64_tr_b16 v[154:155], %[lo] offset:37376\n\t"
    "v_mfma_f32_32x32x16_bf16 v[128:143], v[156:159], v[60:63], v[128:143]\n\t"
    "ds_read_b64_tr_b16 v[156:157], %[lo] offset:38912\n\t"
    "ds_read_b64_tr_b16 v[158:159], %[lo] offset:39424\n\t"
    "s_waitcnt lgkmcnt(4)\n\t"
    "v_mfma_f32_32x32x16_bf16 v[128:143], v[144:147], v[64:67], v[128:143]\n\t"
    "ds_read_b64_tr_b16 v[144:145], %[lo] offset:40960\n\t"
    "ds_read_b64_tr_b16 v[146:147], %[lo] offset:41472\n\t"
    "v_mfma_f32_32x32x16_bf16 v[128:143], v[148:151], v[68:71], v[128:143]\n\t"
    "ds_read_b64_tr_b16 v[148:149], %[lo] offset:43008\n\t"
    "ds_read_b64_tr_b16 v[150:151], %[lo] offset:43520\n\t"
    "s_waitcnt lgkmcnt(4)\n\t"
    "v_mfma_f32_32x32x16_bf16 v[128:143], v[152:155], v[72:75], v[128:143]\n\t"
    "ds_read_b64_tr_b16 v[152:153], %[lo] offset:45056\n\t"
    "ds_read_b64_tr_b16 v[154:155], %[lo] offset:45568\n\t"
    "v_mfma_f32_32x32x16_bf16 v[128:143], v[156:159], v[76:79], v[128:143]\n\t"
    "ds_read_b64_tr_b16 v[156:157], %[lo] offset:47104\n\t"
    "ds_read_b64_tr_b16 v[158:159], %[lo] offset:47616\n\t"
    "s_waitcnt lgkmcnt(4)\n\t"
    "v_mfma_f32_32x32x16_bf16 v[128:143], v[144:147], v[80:83], v[128:143]\n\t"
    "ds_read_b64_tr_b16 v[144:145], %[lo] offset:49152\n\t"
    "ds_read_b64_tr_b16 v[146:147], %[lo] offset:49664\n\t"
    "v_mfma_f32_32x32x16_bf16 v[128:143], v[148:151], v[84:87], v[128:143]\n\t"
    "ds_read_b64_tr_b16 v[148:149], %[lo] offset:51200\n\t"
    "ds_read_b64_tr_b16 v[150:151], %[lo] offset:51712\n\t"
    "s_waitcnt lgkmcnt(4)\n\t"
    "v_mfma_f32_32x32x16_bf16 v[128:143], v[152:155], v[88:91], v[128:143]\n\t"
    "ds_read_b64_tr_b16 v[152:153], %[lo] offset:53248\n\t"
    "ds_read_b64_tr_b16 v[154:155], %[lo] offset:53760\n\t"
    "v_mfma_f32_32x32x16_bf16 v[128:143], v[156:159], v[92:95], v[128:143]\n\t"
    "ds_read_b64_tr_b16 v[156:157], %[lo] offset:55296\n\t"
    "ds_read_b64_tr_b16 v[158:159], %[lo] offset:55808\n\t"
    "s_waitcnt lgkmcnt(4)\n\t"
    "v_mfma_f32_32x32x16_bf16 v[128:143], v[144:147], v[96:99], v[128:143]\n\t"
    "v_mfma_f32_32x32x16_bf16 v[128:143], v[148:151], v[100:103], v[128:143]\n\t"
    "s_waitcnt lgkmcnt(0)\n\t"
    "v_mfma_f32_32x32x16_bf16 v[128:143], v[152:155], v[104:107], v[128:143]\n\t"
    "v_mfma_f32_32x32x16_bf16 v[128:143], v[156:159], v[108:111], v[128:143]\n\t"
    "s_setprio 1\n\t"
    "s_nop 11\n\t"
    "v_mul_f32 v144, 0xbfb8aa3b, v128\n\t"
    "v_mul_f32 v145, 0xbfb8aa3b, v129\n\t"
    "v_mul_f32 v146, 0xbfb8aa3b, v130\n\t"
    "v_mul_f32 v147, 0xbfb8aa3b, v131\n\t"
    "v_mul_f32 v148, 0xbfb8aa3b, v132\n\t"
    "v_mul_f32 v149, 0xbfb8aa3b, v133\n\t"
    "v_mul_f32 v150, 0xbfb8aa3b, v134\n\t"
    "v_mul_f32 v151, 0xbfb8aa3b, v135\n\t"
    "v_exp_f32 v144, v144\n\t"
    "v_exp_f32 v145, v145\n\t"
    "v_exp_f32 v146, v146\n\t"
    "v_exp_f32 v147, v147\n\t"
    "v_exp_f32 v148, v148\n\t"
    "v_exp_f32 v149, v149\n\t"
    "v_exp_f32 v150, v150\n\t"
    "v_exp_f32 v151, v151\n\t"
    "v_add_f32 v144, 1.0, v144\n\t"
    "v_add_f32 v145, 1.0, v145\n\t"
    "v_add_f32 v146, 1.0, v146\n\t"
    "v_add_f32 v147, 1.0, v147\n\t"
    "v_add_f32 v148, 1.0, v148\n\t"
    "v_add_f32 v149, 1.0, v149\n\t"
    "v_add_f32 v150, 1.0, v150\n\t"
    "v_add_f32 v151, 1.0, v151\n\t"
    "v_rcp_f32 v144, v144\n\t"
    "v_rcp_f32 v145, v145\n\t"
    "v_rcp_f32 v146, v146\n\t"
    "v_rcp_f32 v147, v147\n\t"
    "v_rcp_f32 v148, v148\n\t"
    "v_rcp_f32 v149, v149\n\t"
    "v_rcp_f32 v150, v150\n\t"
    "v_rcp_f32 v151, v151\n\t"
    "v_mul_f32 v144, v128, v144\n\t"
    "v_mul_f32 v145, v129, v145\n\t"
    "v_mul_f32 v146, v130, v146\n\t"
    "v_mul_f32 v147, v131, v147\n\t"
    "v_mul_f32 v148, v132, v148\n\t"
    "v_mul_f32 v149, v133, v149\n\t"
    "v_mul_f32 v150, v134, v150\n\t"
    "v_mul_f32 v151, v135, v151\n\t"
    "v_cvt_pk_bf16_f32 v128, v144, v145\n\t"
    "v_cvt_pk_bf16_f32 v129, v146, v147\n\t"
    "v_cvt_pk_bf16_f32 v130, v148, v149\n\t"
    "v_cvt_pk_bf16_f32 v131, v150, v151\n\t"
    "buffer_store_dwordx4 v[128:131], %[vo], %[rs], %[so] offen offset:2048 sc1\n\t"
    "v_mul_f32 v144, 0xbfb8aa3b, v136\n\t"
    "v_mul_f32 v145, 0xbfb8aa3b, v137\n\t"
    "v_mul_f32 v146, 0xbfb8aa3b, v138\n\t"
    "v_mul_f32 v147, 0xbfb8aa3b, v139\n\t"
    "v_mul_f32 v148, 0xbfb8aa3b, v140\n\t"
    "v_mul_f32 v149, 0xbfb8aa3b, v141\n\t"
    "v_mul_f32 v150, 0xbfb8aa3b, v142\n\t"
    "v_mul_f32 v151, 0xbfb8aa3b, v143\n\t"
    "v_exp_f32 v144, v144\n\t"
    "v_exp_f32 v145, v145\n\t"
    "v_exp_f32 v146, v146\n\t"
    "v_exp_f32 v147, v147\n\t"
    "v_exp_f32 v148, v148\n\t"
    "v_exp_f32 v149, v149\n\t"
    "v_exp_f32 v150, v150\n\t"
    "v_exp_f32 v151, v151\n\t"
    "v_add_f32 v144, 1.0, v144\n\t"
    "v_add_f32 v145, 1.0, v145\n\t"
    "v_add_f32 v146, 1.0, v146\n\t"
    "v_add_f32 v147, 1.0, v147\n\t"
    "v_add_f32 v148, 1.0, v148\n\t"
    "v_add_f32 v149, 1.0, v149\n\t"
    "v_add_f32 v150, 1.0, v150\n\t"
    "v_add_f32 v151, 1.0, v151\n\t"
    "v_rcp_f32 v144, v144\n\t"
    "v_rcp_f32 v145, v145\n\t"
    "v_rcp_f32 v146, v146\n\t"
    "v_rcp_f32 v147, v147\n\t"
    "v_rcp_f32 v148, v148\n\t"
    "v_rcp_f32 v149, v149\n\t"
    "v_rcp_f32 v150, v150\n\t"
    "v_rcp_f32 v151, v151\n\t"
    "v_mul_f32 v144, v136, v144\n\t"
    "v_mul_f32 v145, v137, v145\n\t"
    "v_mul_f32 v146, v138, v146\n\t"
    "v_mul_f32 v147, v139, v147\n\t"
    "v_mul_f32 v148, v140, v148\n\t"
    "v_mul_f32 v149, v141, v149\n\t"
    "v_mul_f32 v150, v142, v150\n\t"
    "v_mul_f32 v151, v143, v151\n\t"
    "v_cvt_pk_bf16_f32 v132, v144, v145\n\t"
    "v_cvt_pk_bf16_f32 v133, v146, v147\n\t"
    "v_cvt_pk_bf16_f32 v134, v148, v149\n\t"
    "v_cvt_pk_bf16_f32 v135, v150, v151\n\t"
    "buffer_store_dwordx4 v[132:135], %[vo], %[rs], %[so] offen offset:3072 sc1\n\t"
    "s_nop 1\n\t"
    : [lo] "+v"(lo)
    : "{v[0:15]}"(xv[0]), "{v[16:31]}"(xv[1]), "{v[32:47]}"(xv[2]), "{v[48:63]}"(xv[3]), "{v[64:79]}"(xv[4]), "{v[80:95]}"(xv[5]), "{v[96:111]}"(xv[6]),
      [vo] "v"(vo), [rs] "s"(rs), [so] "s"(so)
    : "memory", "v112", "v113", "v114", "v115", "v116", "v117", "v118", "v119", "v120", "v121", "v122", "v123", "v124", "v125", "v126", "v127", "v128", "v129", "v130", "v131", "v132", "v133", "v134", "v135", "v136", "v137", "v138", "v139", "v140", "v141", "v142", "v143", "v144", "v145", "v146", "v147", "v148", "v149", "v150", "v151", "v152", "v153", "v154", "v155", "v156", "v157", "v158", "v159", "v160", "v161", "v162", "v163");
}
DI void q_proj(const u32x16 (&xv)[7], f32x16& q, unsigned lo) {
    asm volatile(
    "s_setprio 2\n\t"
    "s_nop 4\n\t"
    "ds_read_b64_tr_b16 v[144:145], %[lo] offset:40960\n\t"
    "ds_read_b64_tr_b16 v[146:147], %[lo] offset:41216\n\t"
    "ds_read_b64_tr_b16 v[148:149], %[lo] offset:41984\n\t"
    "ds_read_b64_tr_b16 v[150:151], %[lo] offset:42240\n\t"
    "ds_read_b64_tr_b16 v[152:153], %[lo] offset:43008\n\t"
    "ds_read_b64_tr_b16 v[154:155], %[lo] offset:43264\n\t"
    "ds_read_b64_tr_b16 v[156:157], %[lo] offset:44032\n\t"
    "ds_read_b64_tr_b16 v[158:159], %[lo] offset:44288\n\t"
    "s_waitcnt lgkmcnt(4)\n\t"
    "v_mfma_f32_32x32x16_bf16 v[112:127], v[144:147], v[0:3], 0\n\t"
    "ds_read_b64_tr_b16 v[144:145], %[lo] offset:8192\n\t"
    "ds_read_b64_tr_b16 v[146:147], %[lo] offset:8448\n\t"
    "v_mfma_f32_32x32x16_bf16 v[112:127], v[148:151], v[4:7], v[112:127]\n\t"
    "ds_read_b64_tr_b16 v[148:149], %[lo] offset:9216\n\t"
    "ds_read_b64_tr_b16 v[150:151], %[lo] offset:9472\n\t"
    "s_waitcnt lgkmcnt(4)\n\t"
    "v_mfma_f32_32x32x16_bf16 v[112:127], v[152:155], v[8:11], v[112:127]\n\t"
    "ds_read_b64_tr_b16 v[152:153], %[lo] offset:10240\n\t"
    "ds_read_b64_tr_b16 v[154:155], %[lo] offset:10496\n\t"
    "v_mfma_f32_32x32x16_bf16 v[112:127], v[156:159], v[12:15], v[112:127]\n\t"
    "ds_read_b64_tr_b16 v[156:157], %[lo] offset:11264\n\t"
    "ds_read_b64_tr_b16 v[158:159], %[lo] offset:11520\n\t"
    "s_setprio 3\n\t"
    "s_waitcnt lgkmcnt(4)\n\t"
    "v_mfma_f32_32x32x16_bf16 v[112:127], v[144:147], v[16:19], v[112:127]\n\t"
    "ds_read_b64_tr_b16 v[144:145], %[lo] offset:12288\n\t"
    "ds_read_b64_tr_b16 v[146:147], %[lo] offset:12544\n\t"
    "v_mfma_f32_32x32x16_bf16 v[112:127], v[148:151], v[20:23], v[112:127]\n\t"
    "ds_read_b64_tr_b16 v[148:149], %[lo] offset:13312\n\t"
    "ds_read_b64_tr_b16 v[150:151], %[lo] offset:13568\n\t"
    "s_waitcnt lgkmcnt(4)\n\t"
    "v_mfma_f32_32x32x16_bf16 v[112:127], v[152:155], v[24:27], v[112:127]\n\t"
    "ds_read_b64_tr_b16 v[152:153], %[lo] offset:14336\n\t"
    "ds_read_b64_tr_b16 v[154:155], %[lo] offset:14592\n\t"
    "v_mfma_f32_32x32x16_bf16 v[112:127], v[156:159], v[28:31], v[112:127]\n\t"
    "ds_read_b64_tr_b16 v[156:157], %[lo] offset:15360\n\t"
    "ds_read_b64_tr_b16 v[158:159], %[lo] offset:15616\n\t"
    "s_waitcnt lgkmcnt(4)\n\t"
    "v_mfma_f32_32x32x16_bf16 v[112:127], v[144:147], v[32:35], v[112:127]\n\t"
    "ds_read_b64_tr_b16 v[144:145], %[lo] offset:16384\n\t"
    "ds_read_b64_tr_b16 v[146:147], %[lo] offset:16640\n\t"
    "v_mfma_f32_32x32x16_bf16 v[112:127], v[148:151], v[36:39], v[112:127]\n\t"
    "ds_read_b64_tr_b16 v[148:149], %[lo] offset:17408\n\t"
    "ds_read_b64_tr_b16 v[150:151], %[lo] offset:17664\n\t"
    "s_waitcnt lgkmcnt(4)\n\t"
    "v_mfma_f32_32x32x16_bf16 v[112:127], v[152:155], v[40:43], v[112:127]\n\t"
    "ds_read_b64_tr_b16 v[152:153], %[lo] offset:18432\n\t"
    "ds_read_b64_tr_b16 v[154:155], %[lo] offset:18688\n\t"
    "v_mfma_f32_32x32x16_bf16 v[112:127], v[156:159], v[44:47], v[112:127]\n\t"
    "ds_read_b64_tr_b16 v[156:157], %[lo] offset:19456\n\t"
    "ds_read_b64_tr_b16 v[158:159], %[lo] offset:19712\n\t"
    "s_waitcnt lgkmcnt(4)\n\t"
    "v_mfma_f32_32x32x16_bf16 v[112:127], v[144:147], v[48:51], v[112:127]\n\t"
    "ds_read_b64_tr_b16 v[144:145], %[lo] offset:20480\n\t"
    "ds_read_b64_tr_b16 v[146:147], %[lo] offset:20736\n\t"
    "v_mfma_f32_32x32x16_bf16 v[112:127], v[148:151], v[52:55], v[112:127]\n\t"
    "ds_read_b64_tr_b16 v[148:149], %[lo] offset:21504\n\t"
    "ds_read_b64_tr_b16 v[150:151], %[lo] offset:21760\n\t"
    "s_waitcnt lgkmcnt(4)\n\t"
    "v_mfma_f32_32x32x16_bf16 v[112:127], v[152:155], v[56:59], v[112:127]\n\t"
    "ds_read_b64_tr_b16 v[152:153], %[lo] offset:22528\n\t"
    "ds_read_b64_tr_b16 v[154:155], %[lo] offset:22784\n\t"
    "v_mfma_f32_32x32x16_bf16 v[112:127], v[156:159], v[60:63], v[112:127]\n\t"
    "ds_read_b64_tr_b16 v[156:157], %[lo] offset:23552\n\t"
    "ds_read_b64_tr_b16 v[158:159], %[lo] offset:23808\n\t"
    "s_waitcnt lgkmcnt(4)\n\t"
    "v_mfma_f32_32x32x16_bf16 v[112:127], v[144:147], v[64:67], v[112:127]\n\t"
    "ds_read_b64_tr_b16 v[144:145], %[lo] offset:24576\n\t"
    "ds_read_b64_tr_b16 v[146:147], %[lo] offset:24832\n\t"
    "v_mfma_f32_32x32x16_bf16 v[112:127], v[148:151], v[68:71], v[112:127]\n\t"
    "ds_read_b64_tr_b16 v[148:149], %[lo] offset:25600\n\t"
    "ds_read_b64_tr_b16 v[150:151], %[lo] offset:25856\n\t"
    "s_waitcnt lgkmcnt(4)\n\t"
    "v_mfma_f32_32x32x16_bf16 v[112:127], v[152:155], v[72:75], v[112:127]\n\t"
    "ds_read_b64_tr_b16 v[152:153], %[lo] offset:26624\n\t"
    "ds_read_b64_tr_b16 v[154:155], %[lo] offset:26880\n\t"
    "v_mfma_f32_32x32x16_bf16 v[112:127], v[156:159], v[76:79], v[112:127]\n\t"
    "ds_read_b64_tr_b16 v[156:157], %[lo] offset:27648\n\t"
    "ds_read_b64_tr_b16 v[158:159], %[lo] offset:27904\n\t"
    "s_waitcnt lgkmcnt(4)\n\t"
    "v_mfma_f32_32x32x16_bf16 v[112:127], v[144:147], v[80:83], v[112:127]\n\t"
    "ds_read_b64_tr_b16 v[144:145], %[lo] offset:28672\n\t"
    "ds_read_b64_tr_b16 v[146:147], %[lo] offset:28928\n\t"
    "v_mfma_f32_32x32x16_bf16 v[112:127], v[148:151], v[84:87], v[112:127]\n\t"
    "ds_read_b64_tr_b16 v[148:149], %[lo] offset:29696\n\t"
    "ds_read_b64_tr_b16 v[150:151], %[lo] offset:29952\n\t"
    "s_waitcnt lgkmcnt(4)\n\t"
    "v_mfma_f32_32x32x16_bf16 v[112:127], v[152:155], v[88:91], v[112:127]\n\t"
    "ds_read_b64_tr_b16 v[152:153], %[lo] offset:30720\n\t"
    "ds_read_b64_tr_b16 v[154:155], %[lo] offset:30976\n\t"
    "v_mfma_f32_32x32x16_bf16 v[112:127], v[156:159], v[92:95], v[112:127]\n\t"
    "ds_read_b64_tr_b16 v[156:157], %[lo] offset:31744\n\t"
    "ds_read_b64_tr_b16 v[158:159], %[lo] offset:32000\n\t"
    "s_waitcnt lgkmcnt(4)\n\t"
    "v_mfma_f32_32x32x16_bf16 v[112:127], v[144:147], v[96:99], v[112:127]\n\t"
    "v_mfma_f32_32x32x16_bf16 v[112:127], v[148:151], v[100:103], v[112:127]\n\t"
    "s_waitcnt lgkmcnt(0)\n\t"
    "v_mfma_f32_32x32x16_bf16 v[112:127], v[152:155], v[104:107], v[112:127]\n\t"
    "v_mfma_f32_32x32x16_bf16 v[112:127], v[156:159], v[108:111], v[112:127]\n\t"
    "s_nop 11\n\t"
    : "=&{v[112:127]}"(q)
    : "{v[0:15]}"(xv[0]), "{v[16:31]}"(xv[1]), "{v[32:47]}"(xv[2]), "{v[48:63]}"(xv[3]), "{v[64:79]}"(xv[4]), "{v[80:95]}"(xv[5]), "{v[96:111]}"(xv[6]), [lo] "v"(lo)
    : "memory", "v144", "v145", "v146", "v147", "v148", "v149", "v150", "v151", "v152", "v153", "v154", "v155", "v156", "v157", "v158", "v159");
}
DI void q_store(f32x16 c, char* lds, int h, int rb) {
    const int lane = lane_id(), hh = lane >> 5;
    const float qs = 0.18898223650461362f * LOG2E;
    const bool odd = h & 1;
#pragma unroll
    for (int i = 0; i < 16; ++i) {
        const bool isp = odd ? (hh == 0 && i < 4) : (hh == 1 && i >= 12);
        c[i] = isp ? 0.f : c[i] * qs;
    }
    LAS bf16x8* qd = (LAS bf16x8*)((lds_ptr)lds + L_Q) + rb * 128 + lane;
    qd[0] = pack8(c, 0); qd[64] = pack8(c, 1);
    lds_arrive(lds, C_Q);
}

DI void load_part(const Ptrs& P, char* lds, int h, int j, int t, int fw) {
    const unsigned l0 = (unsigned)(uintptr_t)lds;
    const int lane = lane_id(), l3 = lane >> 3;
    const unsigned wch = ((lane & 7) ^ (((l3 >> 1) & 1) << 2)) * 16;
    const int cs = (h * DH) & ~7, hc = j < 14 ? j : 0;
    const unsigned xo0 = l3 * (DM * 2) + ((lane & 7) ^ ((lane >> 4) & 7)) * 16, xo1 = (8 + l3) * (DM * 2) + ((lane & 7) ^ ((4 + (lane >> 4)) & 7)) * 16;
    const unsigned ko = l3 * (NQKV * 2) + wch, qo = (lane >> 2) * (NQKV * 2) + (lane & 3) * 16, fo = l3 * (FF * 2) + wch;
    const u16* xs = P.x + (size_t)(t * 128 + 32 * fw) * DM;
    const u16* wk = P.wqkv + (size_t)(16 * fw) * NQKV + DM + 64 * hc;
    const u16* wq = P.wqkv + (size_t)(16 * fw) * NQKV + cs;
    const u16* wf = P.w1 + (size_t)(16 * fw) * FF + 128 * hc;
    auto dma_x = [&](int c) {
        const unsigned d = l0 + c * 16384 + fw * 4096;
        glds16s(xo0, xs + c * 64, d); glds16s(xo1, xs + c * 64, d + 1024);
        glds16s(xo0, xs + c * 64 + 16 * DM, d + 2048); glds16s(xo1, xs + c * 64 + 16 * DM, d + 3072);
    };
    auto dma_k = [&](int c) {
        const unsigned d = l0 + kv_off(c) + fw * 2048; const u16* s = wk + (size_t)c * 64 * NQKV;
        glds16s(ko, s, d); glds16s(ko, s + 8 * NQKV, d + 1024);
    };
    auto dma_q = [&](int c) { glds16s(qo, wq + (size_t)c * 64 * NQKV, l0 + wq_off(c) + fw * 1024); };
    auto dma_u = [&](int n) {
        const unsigned d = l0 + n * 8192 + fw * 2048; const u16* s = wf + (size_t)(n % 7) * 64 * FF + (n / 7) * 64;
        glds16s(fo, s, d); glds16s(fo, s + 8 * FF, d + 1024);
    };
    dma_x(2); dma_k(2);
    const unsigned bid = blockIdx.x, ul = lane & 31, q0 = (bid * 49u) >> 2;
    {
        const unsigned qi = q0 + min(ul & 15u, 12u);
        const unsigned ox = (t * 128 + h * 8 + (ul >> 2)) * (DM * 2) + (3 + (ul & 3)) * 128;
        const unsigned ok = ul < 16u ? (192 + t * 16 + ul) * (NQKV * 2) + (7 + hc) * 128 : (qi / 7u) * (NQKV * 2u - 896u) + qi * 128u;
        const unsigned ou = (bid * 49u + (fw & 1) * 24u + min(ul, 24u)) * 128u;
        const char* base = (const char*)(fw == 0 ? P.x : fw == 1 ? P.wqkv : P.w1);
        touch32(fw == 0 ? ox : fw == 1 ? ok : ou, base, l0 + L_SINK);
    }
    dma_x(3); dma_k(3);
    wait_bar<15>(); dma_x(4); dma_k(4);
    wait_bar<19>(); dma_x(5); dma_k(5);
    wait_bar<19>(); dma_x(6); dma_k(6);
    touch32(fw == 3 ? (q0 + min(ul, 12u)) * 128u : (bid * 49u + fw * 16u + min(ul, 16u)) * 128u, (const char*)(fw == 3 ? P.wo : P.w2), l0 + L_SINK);
    wait_bar<19>(); dma_u(0); dma_u(1); dma_u(2);
    wait_bar<19>(); dma_u(3); dma_u(4); dma_u(5);
    wait_bar<19>(); dma_u(6); dma_u(7); dma_u(8);
    wait_bar<19>(); dma_u(9); dma_u(10); dma_u(11);
    lds_wait(lds, C_X6, 8);
    dma_u(12); dma_u(13);
    wait_vm<14>(); lds_arrive(lds, C_W1A);
    dma_q(0); dma_q(1); dma_q(2);
    wait_vm<3>(); lds_arrive(lds, C_W1B);
    dma_q(3); dma_q(4); dma_q(5); dma_q(6);
    wait_vm<0>(); lds_arrive(lds, C_WQ);
}

DI void attn_loop(f32x16& own, unsigned qc, unsigned qa, unsigned vk, unsigned vv, u64 vm, u32x4 kvrs, const int (&ms)[4], unsigned tv) {
    unsigned so, tq;
    asm volatile(
    "s_nop 4\n\t"
    "buffer_load_dwordx4 v[128:131], %[vk], %[rs], 0 offen offset:0 sc1\n\t"
    "buffer_load_dwordx4 v[132:135], %[vk], %[rs], 0 offen offset:1024 sc1\n\t"
    "v_mov_b32 v136, %[tv]\n\t"
    "v_mov_b32 v137, %[tv]\n\t"
    "v_mov_b32 v138, %[tv]\n\t"
    "v_mov_b32 v139, %[tv]\n\t"
    "v_mov_b32 v140, %[tv]\n\t"
    "v_mov_b32 v141, %[tv]\n\t"
    "v_mov_b32 v142, %[tv]\n\t"
    "v_mov_b32 v143, %[tv]\n\t"
    "v_mov_b32 v152, %[tv]\n\t"
    "v_mov_b32 v153, %[tv]\n\t"
    "v_mov_b32 v154, %[tv]\n\t"
    "v_mov_b32 v155, %[tv]\n\t"
    "v_mov_b32 v156, %[tv]\n\t"
    "v_mov_b32 v157, %[tv]\n\t"
    "v_mov_b32 v158, %[tv]\n\t"
    "v_mov_b32 v159, %[tv]\n\t"
    "s_mov_b64 exec, %[vm]\n\t"
    "buffer_load_dwordx4 v[136:139], %[vv], %[rs], 0 offen offset:0 sc1\n\t"
    "buffer_load_dwordx4 v[140:143], %[vv], %[rs], 0 offen offset:256 sc1\n\t"
    "s_mov_b64 exec, -1\n\t"
    "s_mov_b32 %[so], %[kr]\n\t"
    "buffer_load_dwordx4 v[144:147], %[vk], %[rs], %[so] offen offset:0 sc1\n\t"
    "buffer_load_dwordx4 v[148:151], %[vk], %[rs], %[so] offen offset:1024 sc1\n\t"
    "v_mov_b64 v[0:1], 0\n\t"
    "v_mov_b64 v[2:3], 0\n\t"
    "v_mov_b64 v[4:5], 0\n\t"
    "v_mov_b64 v[6:7], 0\n\t"
    "v_mov_b64 v[8:9], 0\n\t"
    "v_mov_b64 v[10:11], 0\n\t"
    "v_mov_b64 v[12:13], 0\n\t"
    "v_mov_b64 v[14:15], 0\n\t"
    "v_mov_b64 v[16:17], 0\n\t"
    "v_mov_b64 v[18:19], 0\n\t"
    "v_mov_b64 v[20:21], 0\n\t"
    "v_mov_b64 v[22:23], 0\n\t"
    "v_mov_b64 v[24:25], 0\n\t"
    "v_mov_b64 v[26:27], 0\n\t"
    "v_mov_b64 v[28:29], 0\n\t"
    "v_mov_b64 v[30:31], 0\n\t"
    "v_mov_b64 v[32:33], 0\n\t"
    "v_mov_b64 v[34:35], 0\n\t"
    "v_mov_b64 v[36:37], 0\n\t"
    "v_mov_b64 v[38:39], 0\n\t"
    "v_mov_b64 v[40:41], 0\n\t"
    "v_mov_b64 v[42:43], 0\n\t"
    "v_mov_b64 v[44:45], 0\n\t"
    "v_mov_b64 v[46:47], 0\n\t"
    "v_mov_b64 v[48:49], 0\n\t"
    "v_mov_b64 v[50:51], 0\n\t"
    "v_mov_b64 v[52:53], 0\n\t"
    "v_mov_b64 v[54:55], 0\n\t"
    "v_mov_b64 v[56:57], 0\n\t"
    "v_mov_b64 v[58:59], 0\n\t"
    "v_mov_b64 v[60:61], 0\n\t"
    "v_mov_b64 v[62:63], 0\n\t"
    "5:\n\t"
    "ds_read_b32 v96, %[qc]\n\t"
    "s_waitcnt lgkmcnt(0)\n\t"
    "v_readfirstlane_b32 %[tq], v96\n\t"
    "s_cmp_ge_u32 %[tq], 4\n\t"
    "s_cbranch_scc1 6f\n\t"
    "s_sleep 1\n\t"
    "s_branch 5b\n\t"
    "6:\n\t"
    "v_xor_b32 v96, 0, %[qa]\n\t"
    "v_xor_b32 v97, 2048, %[qa]\n\t"
    "v_xor_b32 v98, 4096, %[qa]\n\t"
    "v_xor_b32 v99, 6144, %[qa]\n\t"
    "ds_read_b128 v[64:67], v96 offset:0\n\t"
    "ds_read_b128 v[68:71], v96 offset:1024\n\t"
    "ds_read_b128 v[72:75], v97 offset:0\n\t"
    "ds_read_b128 v[76:79], v97 offset:1024\n\t"
    "ds_read_b128 v[80:83], v98 offset:0\n\t"
    "ds_read_b128 v[84:87], v98 offset:1024\n\t"
    "ds_read_b128 v[88:91], v99 offset:0\n\t"
    "ds_read_b128 v[92:95], v99 offset:1024\n\t"
    "s_waitcnt vmcnt(4) lgkmcnt(0)\n\t"
    "v_mfma_f32_32x32x16_bf16 v[96:111], v[128:131], v[64:67], 0\n\t"
    "v_mfma_f32_32x32x16_bf16 v[96:111], v[132:135], v[68:71], v[96:111]\n\t"
    "s_cmp_eq_u32 0, 0\n\t"
    "s_nop 10\n\t"
    "0:\n\t"
    "ds_read_b32 %[tv], %[qc] offset:12\n\t"
    "v_exp_f32 v96, v96\n\t"
    "v_exp_f32 v97, v97\n\t"
    "s_cbranch_scc1 1f\n\t"
    "v_mfma_f32_32x32x16_bf16 v[48:63], v[156:159], v[120:123], v[48:63]\n\t"
    "1:\n\t"
    "s_mov_b64 exec, %[vm]\n\t"
    "buffer_load_dwordx4 v[152:155], %[vv], %[rs], %[so] offen offset:0 sc1\n\t"
    "buffer_load_dwordx4 v[156:159], %[vv], %[rs], %[so] offen offset:256 sc1\n\t"
    "s_mov_b64 exec, -1\n\t"
    "s_add_u32 %[so], %[so], %[kr]\n\t"
    "v_exp_f32 v98, v98\n\t"
    "v_exp_f32 v99, v99\n\t"
    "v_cvt_pk_bf16_f32 v96, v96, v97\n\t"
    "v_cvt_pk_bf16_f32 v97, v98, v99\n\t"
    "v_mfma_f32_32x32x16_bf16 v[112:127], v[128:131], v[72:75], 0\n\t"
    "v_exp_f32 v100, v100\n\t"
    "v_exp_f32 v101, v101\n\t"
    "v_exp_f32 v102, v102\n\t"
    "v_exp_f32 v103, v103\n\t"
    "v_cvt_pk_bf16_f32 v98, v100, v101\n\t"
    "v_cvt_pk_bf16_f32 v99, v102, v103\n\t"
    "v_mfma_f32_32x32x16_bf16 v[112:127], v[132:135], v[76:79], v[112:127]\n\t"
    "v_exp_f32 v104, v104\n\t"
    "v_exp_f32 v105, v105\n\t"
    "v_exp_f32 v106, v106\n\t"
    "v_exp_f32 v107, v107\n\t"
    "v_cvt_pk_bf16_f32 v104, v104, v105\n\t"
    "v_cvt_pk_bf16_f32 v105, v106, v107\n\t"
    "s_waitcnt vmcnt(4)\n\t"
    "v_mfma_f32_32x32x16_bf16 v[0:15], v[136:139], v[96:99], v[0:15]\n\t"
    "v_exp_f32 v108, v108\n\t"
    "v_exp_f32 v109, v109\n\t"
    "v_exp_f32 v110, v110\n\t"
    "v_exp_f32 v111, v111\n\t"
    "v_cvt_pk_bf16_f32 v106, v108, v109\n\t"
    "v_cvt_pk_bf16_f32 v107, v110, v111\n\t"
    "v_exp_f32 v112, v112\n\t"
    "v_exp_f32 v113, v113\n\t"
    "v_mfma_f32_32x32x16_bf16 v[0:15], v[140:143], v[104:107], v[0:15]\n\t"
    "v_exp_f32 v114, v114\n\t"
    "v_exp_f32 v115, v115\n\t"
    "v_cvt_pk_bf16_f32 v112, v112, v113\n\t"
    "v_cvt_pk_bf16_f32 v113, v114, v115\n\t"
    "v_mfma_f32_32x32x16_bf16 v[96:111], v[128:131], v[80:83], 0\n\t"
    "v_exp_f32 v116, v116\n\t"
    "v_exp_f32 v117, v117\n\t"
    "v_exp_f32 v118, v118\n\t"
    "v_exp_f32 v119, v119\n\t"
    "v_cvt_pk_bf16_f32 v114, v116, v117\n\t"
    "v_cvt_pk_bf16_f32 v115, v118, v119\n\t"
    "v_mfma_f32_32x32x16_bf16 v[96:111], v[132:135], v[84:87], v[96:111]\n\t"
    "v_exp_f32 v120, v120\n\t"
    "v_exp_f32 v121, v121\n\t"
    "v_exp_f32 v122, v122\n\t"
    "v_exp_f32 v123, v123\n\t"
    "v_cvt_pk_bf16_f32 v120, v120, v121\n\t"
    "v_cvt_pk_bf16_f32 v121, v122, v123\n\t"
    "v_mfma_f32_32x32x16_bf16 v[16:31], v[136:139], v[112:115], v[16:31]\n\t"
    "v_exp_f32 v124, v124\n\t"
    "v_exp_f32 v125, v125\n\t"
    "v_exp_f32 v126, v126\n\t"
    "v_exp_f32 v127, v127\n\t"
    "v_cvt_pk_bf16_f32 v122, v124, v125\n\t"
    "v_cvt_pk_bf16_f32 v123, v126, v127\n\t"
    "v_exp_f32 v96, v96\n\t"
    "v_exp_f32 v97, v97\n\t"
    "v_mfma_f32_32x32x16_bf16 v[16:31], v[140:143], v[120:123], v[16:31]\n\t"
    "v_exp_f32 v98, v98\n\t"
    "v_exp_f32 v99, v99\n\t"
    "v_cvt_pk_bf16_f32 v96, v96, v97\n\t"
    "v_cvt_pk_bf16_f32 v97, v98, v99\n\t"
    "v_mfma_f32_32x32x16_bf16 v[112:127], v[128:131], v[88:91], 0\n\t"
    "v_exp_f32 v100, v100\n\t"
    "v_exp_f32 v101, v101\n\t"
    "v_exp_f32 v102, v102\n\t"
    "v_exp_f32 v103, v103\n\t"
    "v_cvt_pk_bf16_f32 v98, v100, v101\n\t"
    "v_cvt_pk_bf16_f32 v99, v102, v103\n\t"
    "v_mfma_f32_32x32x16_bf16 v[112:127], v[132:135], v[92:95], v[112:127]\n\t"
    "v_exp_f32 v104, v104\n\t"
    "v_exp_f32 v105, v105\n\t"
    "v_exp_f32 v106, v106\n\t"
    "v_exp_f32 v107, v107\n\t"
    "v_cvt_pk_bf16_f32 v104, v104, v105\n\t"
    "v_cvt_pk_bf16_f32 v105, v106, v107\n\t"
    "v_mfma_f32_32x32x16_bf16 v[32:47], v[136:139], v[96:99], v[32:47]\n\t"
    "v_exp_f32 v108, v108\n\t"
    "v_exp_f32 v109, v109\n\t"
    "v_exp_f32 v110, v110\n\t"
    "v_exp_f32 v111, v111\n\t"
    "v_cvt_pk_bf16_f32 v106, v108, v109\n\t"
    "v_cvt_pk_bf16_f32 v107, v110, v111\n\t"
    "v_exp_f32 v112, v112\n\t"
    "v_exp_f32 v113, v113\n\t"
    "v_mfma_f32_32x32x16_bf16 v[32:47], v[140:143], v[104:107], v[32:47]\n\t"
    "buffer_load_dwordx4 v[128:131], %[vk], %[rs], %[so] offen offset:0 sc1\n\t"
    "buffer_load_dwordx4 v[132:135], %[vk], %[rs], %[so] offen offset:1024 sc1\n\t"
    "v_exp_f32 v114, v114\n\t"
    "v_exp_f32 v115, v115\n\t"
    "v_cvt_pk_bf16_f32 v112, v112, v113\n\t"
    "v_cvt_pk_bf16_f32 v113, v114, v115\n\t"
    "s_waitcnt vmcnt(4)\n\t"
    "v_mfma_f32_32x32x16_bf16 v[96:111], v[144:147], v[64:67], 0\n\t"
    "v_exp_f32 v116, v116\n\t"
    "v_exp_f32 v117, v117\n\t"
    "v_exp_f32 v118, v118\n\t"
    "v_exp_f32 v119, v119\n\t"
    "v_cvt_pk_bf16_f32 v114, v116, v117\n\t"
    "v_cvt_pk_bf16_f32 v115, v118, v119\n\t"
    "v_mfma_f32_32x32x16_bf16 v[96:111], v[148:151], v[68:71], v[96:111]\n\t"
    "v_exp_f32 v120, v120\n\t"
    "v_exp_f32 v121, v121\n\t"
    "v_exp_f32 v122, v122\n\t"
    "v_exp_f32 v123, v123\n\t"
    "v_cvt_pk_bf16_f32 v120, v120, v121\n\t"
    "v_cvt_pk_bf16_f32 v121, v122, v123\n\t"
    "v_mfma_f32_32x32x16_bf16 v[48:63], v[136:139], v[112:115], v[48:63]\n\t"
    "v_exp_f32 v124, v124\n\t"
    "v_exp_f32 v125, v125\n\t"
    "v_exp_f32 v126, v126\n\t"
    "v_exp_f32 v127, v127\n\t"
    "v_cvt_pk_bf16_f32 v122, v124, v125\n\t"
    "v_cvt_pk_bf16_f32 v123, v126, v127\n\t"
    "v_exp_f32 v96, v96\n\t"
    "v_exp_f32 v97, v97\n\t"
    "v_mfma_f32_32x32x16_bf16 v[48:63], v[140:143], v[120:123], v[48:63]\n\t"
    "s_mov_b64 exec, %[vm]\n\t"
    "buffer_load_dwordx4 v[136:139], %[vv], %[rs], %[so] offen offset:0 sc1\n\t"
    "buffer_load_dwordx4 v[140:143], %[vv], %[rs], %[so] offen offset:256 sc1\n\t"
    "s_mov_b64 exec, -1\n\t"
    "s_add_u32 %[so], %[so], %[kr]\n\t"
    "v_exp_f32 v98, v98\n\t"
    "v_exp_f32 v99, v99\n\t"
    "v_cvt_pk_bf16_f32 v96, v96, v97\n\t"
    "v_cvt_pk_bf16_f32 v97, v98, v99\n\t"
    "v_mfma_f32_32x32x16_bf16 v[112:127], v[144:147], v[72:75], 0\n\t"
    "v_exp_f32 v100, v100\n\t"
    "v_exp_f32 v101, v101\n\t"
    "v_exp_f32 v102, v102\n\t"
    "v_exp_f32 v103, v103\n\t"
    "v_cvt_pk_bf16_f32 v98, v100, v101\n\t"
    "v_cvt_pk_bf16_f32 v99, v102, v103\n\t"
    "v_mfma_f32_32x32x16_bf16 v[112:127], v[148:151], v[76:79], v[112:127]\n\t"
    "v_exp_f32 v104, v104\n\t"
    "v_exp_f32 v105, v105\n\t"
    "v_exp_f32 v106, v106\n\t"
    "v_exp_f32 v107, v107\n\t"
    "v_cvt_pk_bf16_f32 v104, v104, v105\n\t"
    "v_cvt_pk_bf16_f32 v105, v106, v107\n\t"
    "s_waitcnt vmcnt(4)\n\t"
    "v_mfma_f32_32x32x16_bf16 v[0:15], v[152:155], v[96:99], v[0:15]\n\t"
    "v_exp_f32 v108, v108\n\t"
    "v_exp_f32 v109, v109\n\t"
    "v_exp_f32 v110, v110\n\t"
    "v_exp_f32 v111, v111\n\t"
    "v_cvt_pk_bf16_f32 v106, v108, v109\n\t"
    "v_cvt_pk_bf16_f32 v107, v110, v111\n\t"
    "v_exp_f32 v112, v112\n\t"
    "v_exp_f32 v113, v113\n\t"
    "v_mfma_f32_32x32x16_bf16 v[0:15], v[156:159], v[104:107], v[0:15]\n\t"
    "v_exp_f32 v114, v114\n\t"
    "v_exp_f32 v115, v115\n\t"
    "v_cvt_pk_bf16_f32 v112, v112, v113\n\t"
    "v_cvt_pk_bf16_f32 v113, v114, v115\n\t"
    "v_mfma_f32_32x32x16_bf16 v[96:111], v[144:147], v[80:83], 0\n\t"
    "v_exp_f32 v116, v116\n\t"
    "v_exp_f32 v117, v117\n\t"
    "v_exp_f32 v118, v118\n\t"
    "v_exp_f32 v119, v119\n\t"
    "v_cvt_pk_bf16_f32 v114, v116, v117\n\t"
    "v_cvt_pk_bf16_f32 v115, v118, v119\n\t"
    "v_mfma_f32_32x32x16_bf16 v[96:111], v[148:151], v[84:87], v[96:111]\n\t"
    "v_exp_f32 v120, v120\n\t"
    "v_exp_f32 v121, v121\n\t"
    "v_exp_f32 v122, v122\n\t"
    "v_exp_f32 v123, v123\n\t"
    "v_cvt_pk_bf16_f32 v120, v120, v121\n\t"
    "v_cvt_pk_bf16_f32 v121, v122, v123\n\t"
    "v_mfma_f32_32x32x16_bf16 v[16:31], v[152:155], v[112:115], v[16:31]\n\t"
    "v_exp_f32 v124, v124\n\t"
    "v_exp_f32 v125, v125\n\t"
    "v_exp_f32 v126, v126\n\t"
    "v_exp_f32 v127, v127\n\t"
    "v_cvt_pk_bf16_f32 v122, v124, v125\n\t"
    "v_cvt_pk_bf16_f32 v123, v126, v127\n\t"
    "v_exp_f32 v96, v96\n\t"
    "v_exp_f32 v97, v97\n\t"
    "v_mfma_f32_32x32x16_bf16 v[16:31], v[156:159], v[120:123], v[16:31]\n\t"
    "v_exp_f32 v98, v98\n\t"
    "v_exp_f32 v99, v99\n\t"
    "v_cvt_pk_bf16_f32 v96, v96, v97\n\t"
    "v_cvt_pk_bf16_f32 v97, v98, v99\n\t"
    "v_mfma_f32_32x32x16_bf16 v[112:127], v[144:147], v[88:91], 0\n\t"
    "v_exp_f32 v100, v100\n\t"
    "v_exp_f32 v101, v101\n\t"
    "v_exp_f32 v102, v102\n\t"
    "v_exp_f32 v103, v103\n\t"
    "v_cvt_pk_bf16_f32 v98, v100, v101\n\t"
    "v_cvt_pk_bf16_f32 v99, v102, v103\n\t"
    "v_mfma_f32_32x32x16_bf16 v[112:127], v[148:151], v[92:95], v[112:127]\n\t"
    "v_exp_f32 v104, v104\n\t"
    "v_exp_f32 v105, v105\n\t"
    "v_exp_f32 v106, v106\n\t"
    "v_exp_f32 v107, v107\n\t"
    "v_cvt_pk_bf16_f32 v104, v104, v105\n\t"
    "v_cvt_pk_bf16_f32 v105, v106, v107\n\t"
    "v_mfma_f32_32x32x16_bf16 v[32:47], v[152:155], v[96:99], v[32:47]\n\t"
    "v_exp_f32 v108, v108\n\t"
    "v_exp_f32 v109, v109\n\t"
    "v_exp_f32 v110, v110\n\t"
    "v_exp_f32 v111, v111\n\t"
    "v_cvt_pk_bf16_f32 v106, v108, v109\n\t"
    "v_cvt_pk_bf16_f32 v107, v110, v111\n\t"
    "v_exp_f32 v112, v112\n\t"
    "v_exp_f32 v113, v113\n\t"
    "v_mfma_f32_32x32x16_bf16 v[32:47], v[156:159], v[104:107], v[32:47]\n\t"
    "buffer_load_dwordx4 v[144:147], %[vk], %[rs], %[so] offen offset:0 sc1\n\t"
    "buffer_load_dwordx4 v[148:151], %[vk], %[rs], %[so] offen offset:1024 sc1\n\t"
    "v_exp_f32 v114, v114\n\t"
    "v_exp_f32 v115, v115\n\t"
    "v_cvt_pk_bf16_f32 v112, v112, v113\n\t"
    "v_cvt_pk_bf16_f32 v113, v114, v115\n\t"
    "s_waitcnt vmcnt(4)\n\t"
    "v_mfma_f32_32x32x16_bf16 v[96:111], v[128:131], v[64:67], 0\n\t"
    "v_exp_f32 v116, v116\n\t"
    "v_exp_f32 v117, v117\n\t"
    "v_exp_f32 v118, v118\n\t"
    "v_exp_f32 v119, v119\n\t"
    "v_cvt_pk_bf16_f32 v114, v116, v117\n\t"
    "v_cvt_pk_bf16_f32 v115, v118, v119\n\t"
    "v_mfma_f32_32x32x16_bf16 v[96:111], v[132:135], v[68:71], v[96:111]\n\t"
    "v_exp_f32 v120, v120\n\t"
    "v_exp_f32 v121, v121\n\t"
    "v_exp_f32 v122, v122\n\t"
    "v_exp_f32 v123, v123\n\t"
    "v_cvt_pk_bf16_f32 v120, v120, v121\n\t"
    "v_cvt_pk_bf16_f32 v121, v122, v123\n\t"
    "v_mfma_f32_32x32x16_bf16 v[48:63], v[152:155], v[112:115], v[48:63]\n\t"
    "v_exp_f32 v124, v124\n\t"
    "v_exp_f32 v125, v125\n\t"
    "v_exp_f32 v126, v126\n\t"
    "v_exp_f32 v127, v127\n\t"
    "v_cvt_pk_bf16_f32 v122, v124, v125\n\t"
    "v_cvt_pk_bf16_f32 v123, v126, v127\n\t"
    "s_cmp_lg_u32 %[so], %[sw]\n\t"
    "s_cbranch_scc1 2f\n\t"
    "s_setprio 0\n\t"
    "2:\n\t"
    "s_cmp_eq_u32 %[so], %[k9]\n\t"
    "s_cbranch_scc0 0b\n\t"
    "s_waitcnt lgkmcnt(0)\n\t"
    "v_readfirstlane_b32 %[tq], %[tv]\n\t"
    "v_mfma_f32_32x32x16_bf16 v[48:63], v[156:159], v[120:123], v[48:63]\n\t"
    "v_cvt_pk_bf16_f32 v96, v0, v1\n\t"
    "v_cvt_pk_bf16_f32 v97, v2, v3\n\t"
    "v_cvt_pk_bf16_f32 v98, v4, v5\n\t"
    "v_cvt_pk_bf16_f32 v99, v6, v7\n\t"
    "v_cvt_pk_bf16_f32 v100, v8, v9\n\t"
    "v_cvt_pk_bf16_f32 v101, v10, v11\n\t"
    "v_cvt_pk_bf16_f32 v102, v12, v13\n\t"
    "v_cvt_pk_bf16_f32 v103, v14, v15\n\t"
    "v_add_u32 v64, %[m0], %[vk]\n\t"
    "s_cmp_ge_u32 %[tq], 4\n\t"
    "s_cbranch_scc1 9f\n\t"
    "8:\n\t"
    "s_sleep 1\n\t"
    "ds_read_b32 %[tv], %[qc] offset:12\n\t"
    "s_waitcnt lgkmcnt(0)\n\t"
    "v_readfirstlane_b32 %[tq], %[tv]\n\t"
    "s_cmp_ge_u32 %[tq], 4\n\t"
    "s_cbranch_scc0 8b\n\t"
    "9:\n\t"
    "ds_write_b128 v64, v[96:99]\n\t"
    "ds_write_b128 v64, v[100:103] offset:1024\n\t"
    "v_cvt_pk_bf16_f32 v104, v16, v17\n\t"
    "v_cvt_pk_bf16_f32 v105, v18, v19\n\t"
    "v_cvt_pk_bf16_f32 v106, v20, v21\n\t"
    "v_cvt_pk_bf16_f32 v107, v22, v23\n\t"
    "v_cvt_pk_bf16_f32 v108, v24, v25\n\t"
    "v_cvt_pk_bf16_f32 v109, v26, v27\n\t"
    "v_cvt_pk_bf16_f32 v110, v28, v29\n\t"
    "v_cvt_pk_bf16_f32 v111, v30, v31\n\t"
    "v_add_u32 v65, %[m1], %[vk]\n\t"
    "ds_write_b128 v65, v[104:107]\n\t"
    "ds_write_b128 v65, v[108:111] offset:1024\n\t"
    "v_cvt_pk_bf16_f32 v72, v32, v33\n\t"
    "v_cvt_pk_bf16_f32 v73, v34, v35\n\t"
    "v_cvt_pk_bf16_f32 v74, v36, v37\n\t"
    "v_cvt_pk_bf16_f32 v75, v38, v39\n\t"
    "v_cvt_pk_bf16_f32 v76, v40, v41\n\t"
    "v_cvt_pk_bf16_f32 v77, v42, v43\n\t"
    "v_cvt_pk_bf16_f32 v78, v44, v45\n\t"
    "v_cvt_pk_bf16_f32 v79, v46, v47\n\t"
    "v_add_u32 v66, %[m2], %[vk]\n\t"
    "ds_write_b128 v66, v[72:75]\n\t"
    "ds_write_b128 v66, v[76:79] offset:1024\n\t"
    "s_cmp_lt_i32 %[m3], 0\n\t"
    "s_cbranch_scc1 10f\n\t"
    "v_cvt_pk_bf16_f32 v80, v48, v49\n\t"
    "v_cvt_pk_bf16_f32 v81, v50, v51\n\t"
    "v_cvt_pk_bf16_f32 v82, v52, v53\n\t"
    "v_cvt_pk_bf16_f32 v83, v54, v55\n\t"
    "v_cvt_pk_bf16_f32 v84, v56, v57\n\t"
    "v_cvt_pk_bf16_f32 v85, v58, v59\n\t"
    "v_cvt_pk_bf16_f32 v86, v60, v61\n\t"
    "v_cvt_pk_bf16_f32 v87, v62, v63\n\t"
    "v_add_u32 v67, %[m3], %[vk]\n\t"
    "ds_write_b128 v67, v[80:83]\n\t"
    "ds_write_b128 v67, v[84:87] offset:1024\n\t"
    "10:\n\t"
    "v_mov_b32 %[tv], %[av]\n\t"
    "s_mov_b64 exec, 1\n\t"
    "ds_add_u32 %[qc], %[tv] offset:24\n\t"
    "s_mov_b64 exec, -1\n\t"
    "s_waitcnt vmcnt(0)\n\t"
    : "=&{v[48:63]}"(own), [so] "=&s"(so), [tq] "=&s"(tq), [tv] "+v"(tv)
    : [qc] "v"(qc), [qa] "v"(qa), [vk] "v"(vk), [vv] "v"(vv), [vm] "s"(vm), [rs] "s"(kvrs), [sw] "s"(7 * KVROW), [kr] "n"(KVROW), [k9] "n"(9 * KVROW),
      [m0] "s"(ms[0]), [m1] "s"(ms[1]), [m2] "s"(ms[2]), [m3] "s"(ms[3]), [av] "s"(ms[3] < 0 ? 0x10001u : 1u)
    : "scc", "memory", "v0", "v1", "v2", "v3", "v4", "v5", "v6", "v7", "v8", "v9", "v10", "v11", "v12", "v13", "v14", "v15", "v16", "v17", "v18", "v19", "v20", "v21", "v22", "v23", "v24", "v25", "v26", "v27", "v28", "v29", "v30", "v31", "v32", "v33", "v34", "v35", "v36", "v37", "v38", "v39", "v40", "v41", "v42", "v43", "v44", "v45", "v46", "v47", "v64", "v65", "v66", "v67", "v68", "v69", "v70", "v71", "v72", "v73", "v74", "v75", "v76", "v77", "v78", "v79", "v80", "v81", "v82", "v83", "v84", "v85", "v86", "v87", "v88", "v89", "v90", "v91", "v92", "v93", "v94", "v95", "v96", "v97", "v98", "v99", "v100", "v101", "v102", "v103", "v104", "v105", "v106", "v107", "v108", "v109", "v110", "v111", "v112", "v113", "v114", "v115", "v116", "v117", "v118", "v119", "v120", "v121", "v122", "v123", "v124", "v125", "v126", "v127", "v128", "v129", "v130", "v131", "v132", "v133", "v134", "v135", "v136", "v137", "v138", "v139", "v140", "v141", "v142", "v143", "v144", "v145", "v146", "v147", "v148", "v149", "v150", "v151", "v152", "v153", "v154", "v155", "v156", "v157", "v158", "v159");
}

DI void a_wave(const Ptrs& P, rsrc_t rs, char* lds, int h, int j, int t, int w) {
    char* ws = P.ws;
    u64 tag;
    const int cs = (h * DH) & ~7;
    auto kvf_off = [&](int l) { return WS_FKV + ((((cs + ((l >> 1) & 1) * 31) >> 6) + (l & 4 ? 7 : 0)) * 16 + 2 * w + (l & 1)) * FL; };
    if (w < 4) {
        __builtin_amdgcn_s_setprio(3);
        if (w == 0 && lane_id() < 2) glds4_dev(ws + WS_EP + lane_id() * 4, (unsigned)(uintptr_t)lds + L_EPOCH);
        if (w == 3) {
            const int l = lane_id(), r = l & 31, hh = l >> 5;
            bf16x8 id0, id1;
#pragma unroll
            for (int j = 0; j < 8; ++j) {
                const int kr = 8 * (j >> 2) + 4 * hh + (j & 3);
                id0[j] = (r == kr) ? (short)0x3f80 : (short)0; id1[j] = (r == kr + 16) ? (short)0x3f80 : (short)0;
            }
            *((LAS bf16x8*)((lds_ptr)lds + L_ID) + l) = id0; *((LAS bf16x8*)((lds_ptr)lds + L_ID) + 64 + l) = id1;
        }
        wait_vm<0>();
        u32x16 xv[7];
        if (j < 7) kv_part<false>(xv, rs, lds, j, t, w);
        else if (j < 14) kv_part<true>(xv, rs, lds, j, t, w);
        else { x_only(xv, lds, w); lds_arrive(lds, C_X6); }
        tag = make_tag(lds);
        auto kv_flag = [&] {
            asm volatile("s_waitcnt vmcnt(0)" ::: "memory");
            if (lane_id() == 0 && __hip_atomic_fetch_add((LAS unsigned*)((lds_ptr)lds + L_CTL) + C_KVD, 1u, __ATOMIC_RELAXED, __HIP_MEMORY_SCOPE_WORKGROUP) == 3u)
                __hip_atomic_store((gu64*)(ws + WS_FKV + (j * 16 + t) * FL), tag, RLX_AGENT);
        };
        kv_flag();
        if (j < 14) {
            lds_wait(lds, C_W1B, 4);
            const int l = lane_id();
            ffn_up(xv, (unsigned)(uintptr_t)lds + 57344 + w_lane_off(l, true), l * 16, srd(ws, WS_END), WS_H + (unsigned)((t * 4 + w) * 112 + j * 8 + 4) * 1024u);
        }
        lds_arrive(lds, C_FFN1);
        __builtin_amdgcn_s_setprio(0);
    } else {
        load_part(P, lds, h, j, t, w - 4);
        tag = make_tag(lds);
    }
    const int lane = lane_id();
    const bool odd = h & 1;
    poll_at(ws, kvf_off(lane), 8, tag);
    if (w < 4 && j < 14) publish(ws, WS_FH + ((t * 2 + (w >> 1)) * 16 + j) * FL + (w & 1) * 8 + 16, tag);
    u64 gw = 0;
    if (w < 4) gw = gate_load(ws, t * 128 + w * 32 + (lane & 31));
    const unsigned kvb = WS_KV + (unsigned)w * 8u * KVROW + cs * 64u;
    const unsigned vk = lane * 16, vv = 28672 + ((lane & 31) >> 3) * 512 + (lane >> 5) * 128 + (lane & 7) * 16;
    const u64 kvp = (u64)(uintptr_t)ws + kvb;
    const u32x4 kvrs = {(unsigned)kvp, (unsigned)(kvp >> 32) & 0xffffu, 7u * KVROW + 30720u, 0x00020000u};
    const int qx = 3 ^ (w & 3);
    int ms[4];
#pragma unroll
    for (int i = 0; i < 4; ++i) ms[i] = (int)(uintptr_t)lds + L_MERGE + ((i ^ qx) * 7 + (w < 4 ? i : w - 1)) * 2048;
    if (w < 4) ms[3] = -1;
    unsigned oo = WS_O + (unsigned)(t * 4 + w) * 28672u + cs * 64u, fo = WS_FO + ((t * 2 + (w >> 1)) * 16 + h) * FL + (w & 1) * 8;
    asm volatile("" : "+s"(oo), "+s"(fo));
    f32x16 acc;
    if (w >= 4) __builtin_amdgcn_s_setprio(1);
    attn_loop(acc, (unsigned)(uintptr_t)lds + L_CTL + C_Q * 4, (unsigned)(uintptr_t)lds + L_Q + qx * 2048 + lane * 16, vk, vv,
              odd ? ~0x0000000f0000000full : ~0xf0000000f0000000ull, kvrs, ms, (lane & 31) == (odd ? 0 : 28) ? 0x3f803f80u : 0u);
    __builtin_amdgcn_s_setprio(3);
    bool done = false;
#pragma unroll 1
    for (int attempt = 0; attempt < 2; ++attempt) {
        const int lane = lane_id();
        const int r = lane & 31, hh = lane >> 5;
        bool bad_any = false;
        if (w < 4 && !done) {
            bf16x8 id0 = lds128((lds_cptr)lds + L_ID + lane * 16), id1 = lds128((lds_cptr)lds + L_ID + 1024 + lane * 16);
            float g = gate_get(ws, t * 128 + w * 32 + r, gw, tag);
            lds_cptr mb = (lds_cptr)lds + L_MERGE + w * 14336 + lane * 16;
            PIN(id0); PIN(id1); PIN(g); PIN(mb);
            bf16x8 pf[8];
            while ((lds_wait<0>(lds, C_M1, 0) >> 16) < 4u + attempt * 4u) {}
#pragma unroll
            for (int p = 0; p < 6; ++p) pf[p] = lds128(mb + p * 1024);
            SBAR();
#pragma unroll
            for (int p = 0; p < 6; ++p) acc = MFMA(p & 1 ? id1 : id0, pf[p], acc);
            PIN(acc);
            lds_wait<0>(lds, C_M1, 8 + attempt * 8);
#pragma unroll
            for (int p = 0; p < 8; ++p) pf[p] = lds128(mb + (6 + p) * 1024);
            SBAR();
#pragma unroll
            for (int p = 0; p < 8; ++p) acc = MFMA(p & 1 ? id1 : id0, pf[p], acc);
            const unsigned lw = __float_as_uint(odd ? acc[0] : acc[12]);
            auto ls = __builtin_amdgcn_permlane32_swap(lw, lw, false, false);
            const float l = __uint_as_float(odd ? ls[0] : ls[1]);
            {
                const float rl = g * __builtin_amdgcn_rcpf(l);
                f32x16 y;
#pragma unroll
                for (int i = 0; i < 16; ++i) y[i] = acc[i] * rl;
                u32x4 a = pack8u(y, 0), b = pack8u(y, 1);
                auto s0 = __builtin_amdgcn_permlane32_swap(a[0], a[2], false, false), s1 = __builtin_amdgcn_permlane32_swap(a[1], a[3], false, false);
                auto s2 = __builtin_amdgcn_permlane32_swap(b[0], b[2], false, false), s3 = __builtin_amdgcn_permlane32_swap(b[1], b[3], false, false);
                a = u32x4{s0[0], s1[0], s0[1], s1[1]}; b = u32x4{s2[0], s3[0], s2[1], s3[1]};
                if (odd) {
                    st16(rs, b, lane * 16, oo + 1024);
                    if (lane < 32) __builtin_amdgcn_raw_buffer_store_b64(u32x2{a[2], a[3]}, rs, lane * 16, oo + 8, 16);
                    else st16(rs, a, lane * 16, oo);
                } else {
                    st16(rs, a, lane * 16, oo);
                    if (lane >= 32) __builtin_amdgcn_raw_buffer_store_b64(u32x2{b[0], b[1]}, rs, lane * 16, oo + 1024, 16);
                    else st16(rs, b, lane * 16, oo + 1024);
                }
            }
            PIN(acc);
            float as[4];
#pragma unroll
            for (int i = 0; i < 4; ++i) as[i] = (fabsf(acc[i]) + fabsf(acc[4 + i])) + (fabsf(acc[8 + i]) + fabsf(acc[12 + i]));
            const bool bad = !((l > 1e-18f) & (l < 1e30f) & ((as[0] + as[1]) + (as[2] + as[3]) < 1e37f));
            bad_any = (__ballot(bad) != 0) & (attempt == 0);
            asm volatile("s_waitcnt vmcnt(0)" ::: "memory");
            if (!bad_any) {
                if (lane == 0) __hip_atomic_store((gu64*)(ws + fo), tag, RLX_AGENT);
                done = true;
            }
        }
        if (attempt) break;
        if (w < 4) lds_arrive(lds, C_M2, bad_any ? 0x10001u : 1u);
        if (__builtin_expect(!(lds_wait<4>(lds, C_M2, 4) >> 16), 1)) break;
        bf16x8 qf[4][2];
#pragma unroll
        for (int i = 0; i < 8; ++i) qf[i >> 1][i & 1] = lds128((lds_cptr)lds + L_Q + ((i >> 1) ^ qx) * 2048 + (i & 1) * 1024 + lane * 16);
        f32x16 o[4];
        float mx[4];
#pragma unroll
        for (int i = 0; i < 4; ++i) mx[i] = -INFINITY;
#pragma unroll 1
        for (int kt = 0; kt < 8; ++kt) {
            const bf16x8 a0 = ld16(rs, vk, kvb + kt * KVROW), a1 = ld16(rs, vk, kvb + kt * KVROW + 1024);
#pragma unroll
            for (int i = 0; i < 4; ++i) {
                f32x16 s = MFMA(a0, qf[i][0], f32x16{});
                s = MFMA(a1, qf[i][1], s);
#pragma unroll
                for (int j = 0; j < 16; ++j) mx[i] = fmaxf(mx[i], s[j]);
            }
        }
        LAS float* mxb = (LAS float*)((lds_ptr)lds + L_MX);
#pragma unroll
        for (int i = 0; i < 4; ++i) { mx[i] = fmaxf(mx[i], swap32(mx[i])); if (hh == 0) mxb[w * 128 + (i ^ qx) * 32 + r] = mx[i]; }
        lds_arrive(lds, C_MX);
        lds_wait(lds, C_MX, 8);
#pragma unroll
        for (int i = 0; i < 4; ++i) {
#pragma unroll
            for (int p = 0; p < 8; ++p) mx[i] = fmaxf(mx[i], mxb[p * 128 + (i ^ qx) * 32 + r]);
            o[i] = f32x16{};
        }
        const bool ones = r == (odd ? 0 : 28);
        const bf16x8 one8 = {0x3f80, 0x3f80, 0x3f80, 0x3f80, 0x3f80, 0x3f80, 0x3f80, 0x3f80};
#pragma unroll 1
        for (int kt = 0; kt < 8; ++kt) {
            const bf16x8 a0 = ld16(rs, vk, kvb + kt * KVROW), a1 = ld16(rs, vk, kvb + kt * KVROW + 1024);
            bf16x8 v0 = ld16(rs, vv, kvb + kt * KVROW), v1 = ld16(rs, vv, kvb + kt * KVROW + 256);
            v0 = ones ? one8 : v0; v1 = ones ? one8 : v1;
#pragma unroll
            for (int i = 0; i < 4; ++i) {
                f32x16 s = MFMA(a0, qf[i][0], f32x16{});
                s = MFMA(a1, qf[i][1], s);
#pragma unroll
                for (int j = 0; j < 16; ++j) s[j] = __builtin_amdgcn_exp2f(s[j] - mx[i]);
                o[i] = MFMA(v0, pack8(s, 0), o[i]);
                o[i] = MFMA(v1, pack8(s, 1), o[i]);
            }
        }
#pragma unroll
        for (int i = 0; i < 4; ++i)
            if (ms[i] >= 0) { LAS bf16x8* d = (LAS bf16x8*)(uintptr_t)(ms[i] + lane * 16); d[0] = pack8(o[i], 0); d[64] = pack8(o[i], 1); }
        acc = o[3];
        lds_arrive(lds, C_M1, w < 4 ? 0x10001u : 1u);
    }
}

struct WBase {
    unsigned b[2][3];
    DI bf16x8 frag(int ks, int cb) const {
        const int o = w2_blk(ks) * 8192;
        lds_cptr p = (lds_cptr)(uintptr_t)b[cb][o >> 16] + (o & 65535);
        return trfrag(p, p + 512);
    }
};
struct Ffn2Args { u32x4 rs, r2, ro; unsigned hf, hs, wf, wsp, wo, lb, h0, h1, w0, ca, fo; u64 tag; const char* ws; };
DI void ffn_down(f32x16 (&c)[4], const WBase& wb, const Ffn2Args& A) {
    unsigned sv, t, tv; u64 fv;
    asm volatile(
    "s_mov_b32 %[sv], m0\n\t"
    "s_nop 4\n\t"
    "s_add_u32 %[t], %[hf], 0\n\t"
    "buffer_load_dwordx4 v[64:67], %[h0], %[rs], %[t] offen sc1\n\t"
    "buffer_load_dwordx4 v[68:71], %[h1], %[rs], %[t] offen sc1\n\t"
    "s_add_u32 %[t], %[hf], 1024\n\t"
    "buffer_load_dwordx4 v[72:75], %[h0], %[rs], %[t] offen sc1\n\t"
    "buffer_load_dwordx4 v[76:79], %[h1], %[rs], %[t] offen sc1\n\t"
    "s_add_u32 %[t], %[hf], 2048\n\t"
    "buffer_load_dwordx4 v[80:83], %[h0], %[rs], %[t] offen sc1\n\t"
    "buffer_load_dwordx4 v[84:87], %[h1], %[rs], %[t] offen sc1\n\t"
    "ds_read_b64_tr_b16 v[88:89], %[b00] offset:0\n\t"
    "ds_read_b64_tr_b16 v[90:91], %[b00] offset:512\n\t"
    "ds_read_b64_tr_b16 v[92:93], %[b10] offset:0\n\t"
    "ds_read_b64_tr_b16 v[94:95], %[b10] offset:512\n\t"
    "s_setprio %[pr]\n\t"
    "s_waitcnt vmcnt(4) lgkmcnt(0)\n\t"
    "v_mfma_f32_32x32x16_bf16 v[0:15], v[88:91], v[64:67], 0\n\t"
    "ds_read_b64_tr_b16 v[96:97], %[b00] offset:8192\n\t"
    "ds_read_b64_tr_b16 v[98:99], %[b00] offset:8704\n\t"
    "ds_read_b64_tr_b16 v[100:101], %[b10] offset:8192\n\t"
    "ds_read_b64_tr_b16 v[102:103], %[b10] offset:8704\n\t"
    "v_mfma_f32_32x32x16_bf16 v[32:47], v[88:91], v[68:71], 0\n\t"
    "s_add_u32 m0, %[lb], 57344\n\t"
    "s_add_u32 %[t], %[wf], 272384\n\t"
    "buffer_load_dwordx4 %[w0], %[r2], %[t] offen lds\n\t"
    "s_add_u32 m0, m0, 1024\n\t"
    "s_nop 0\n\t"
    "buffer_load_dwordx4 %[w1], %[r2], %[t] offen lds\n\t"
    "v_mfma_f32_32x32x16_bf16 v[16:31], v[92:95], v[64:67], 0\n\t"
    "v_mfma_f32_32x32x16_bf16 v[48:63], v[92:95], v[68:71], 0\n\t"
    "s_waitcnt vmcnt(4) lgkmcnt(0)\n\t"
    "v_mfma_f32_32x32x16_bf16 v[0:15], v[96:99], v[72:75], v[0:15]\n\t"
    "ds_read_b64_tr_b16 v[88:89], %[b00] offset:16384\n\t"
    "ds_read_b64_tr_b16 v[90:91], %[b00] offset:16896\n\t"
    "ds_read_b64_tr_b16 v[92:93], %[b10] offset:16384\n\t"
    "ds_read_b64_tr_b16 v[94:95], %[b10] offset:16896\n\t"
    "v_mfma_f32_32x32x16_bf16 v[32:47], v[96:99], v[76:79], v[32:47]\n\t"
    "s_add_u32 m0, %[lb], 65536\n\t"
    "s_add_u32 %[t], %[ws], 0\n\t"
    "buffer_load_dwordx4 %[w0], %[r2], %[t] offen lds\n\t"
    "s_add_u32 m0, m0, 1024\n\t"
    "s_nop 0\n\t"
    "buffer_load_dwordx4 %[w1], %[r2], %[t] offen lds\n\t"
    "v_mfma_f32_32x32x16_bf16 v[16:31], v[100:103], v[72:75], v[16:31]\n\t"
    "s_add_u32 %[t], %[hf], 3072\n\t"
    "buffer_load_dwordx4 v[64:67], %[h0], %[rs], %[t] offen sc1\n\t"
    "buffer_load_dwordx4 v[68:71], %[h1], %[rs], %[t] offen sc1\n\t"
    "v_mfma_f32_32x32x16_bf16 v[48:63], v[100:103], v[76:79], v[48:63]\n\t"
    "s_waitcnt vmcnt(6) lgkmcnt(0)\n\t"
    "v_mfma_f32_32x32x16_bf16 v[0:15], v[88:91], v[80:83], v[0:15]\n\t"
    "ds_read_b64_tr_b16 v[96:97], %[b00] offset:24576\n\t"
    "ds_read_b64_tr_b16 v[98:99], %[b00] offset:25088\n\t"
    "ds_read_b64_tr_b16 v[100:101], %[b10] offset:24576\n\t"
    "ds_read_b64_tr_b16 v[102:103], %[b10] offset:25088\n\t"
    "v_mfma_f32_32x32x16_bf16 v[32:47], v[88:91], v[84:87], v[32:47]\n\t"
    "s_add_u32 m0, %[lb], 73728\n\t"
    "s_add_u32 %[t], %[ws], 14336\n\t"
    "buffer_load_dwordx4 %[w0], %[r2], %[t] offen lds\n\t"
    "s_add_u32 m0, m0, 1024\n\t"
    "s_nop 0\n\t"
    "buffer_load_dwordx4 %[w1], %[r2], %[t] offen lds\n\t"
    "v_mfma_f32_32x32x16_bf16 v[16:31], v[92:95], v[80:83], v[16:31]\n\t"
    "s_add_u32 %[t], %[hf], 8192\n\t"
    "buffer_load_dwordx4 v[72:75], %[h0], %[rs], %[t] offen sc1\n\t"
    "buffer_load_dwordx4 v[76:79], %[h1], %[rs], %[t] offen sc1\n\t"
    "v_mfma_f32_32x32x16_bf16 v[48:63], v[92:95], v[84:87], v[48:63]\n\t"
    "s_waitcnt vmcnt(4) lgkmcnt(0)\n\t"
    "v_mfma_f32_32x32x16_bf16 v[0:15], v[96:99], v[64:67], v[0:15]\n\t"
    "ds_read_b64_tr_b16 v[88:89], %[b00] offset:32768\n\t"
    "ds_read_b64_tr_b16 v[90:91], %[b00] offset:33280\n\t"
    "ds_read_b64_tr_b16 v[92:93], %[b10] offset:32768\n\t"
    "ds_read_b64_tr_b16 v[94:95], %[b10] offset:33280\n\t"
    "v_mfma_f32_32x32x16_bf16 v[32:47], v[96:99], v[68:71], v[32:47]\n\t"
    "s_add_u32 m0, %[lb], 81920\n\t"
    "s_add_u32 %[t], %[ws], 28672\n\t"
    "buffer_load_dwordx4 %[w0], %[r2], %[t] offen lds\n\t"
    "s_add_u32 m0, m0, 1024\n\t"
    "s_nop 0\n\t"
    "buffer_load_dwordx4 %[w1], %[r2], %[t] offen lds\n\t"
    "v_mfma_f32_32x32x16_bf16 v[16:31], v[100:103], v[64:67], v[16:31]\n\t"
    "s_add_u32 %[t], %[hf], 9216\n\t"
    "buffer_load_dwordx4 v[80:83], %[h0], %[rs], %[t] offen sc1\n\t"
    "buffer_load_dwordx4 v[84:87], %[h1], %[rs], %[t] offen sc1\n\t"
    "v_mfma_f32_32x32x16_bf16 v[48:63], v[100:103], v[68:71], v[48:63]\n\t"
    "s_waitcnt vmcnt(4) lgkmcnt(0)\n\t"
    "v_mfma_f32_32x32x16_bf16 v[0:15], v[88:91], v[72:75], v[0:15]\n\t"
    "ds_read_b64_tr_b16 v[96:97], %[b00] offset:40960\n\t"
    "ds_read_b64_tr_b16 v[98:99], %[b00] offset:41472\n\t"
    "ds_read_b64_tr_b16 v[100:101], %[b10] offset:40960\n\t"
    "ds_read_b64_tr_b16 v[102:103], %[b10] offset:41472\n\t"
    "v_mfma_f32_32x32x16_bf16 v[32:47], v[88:91], v[76:79], v[32:47]\n\t"
    "s_add_u32 m0, %[lb], 90112\n\t"
    "s_add_u32 %[t], %[ws], 43008\n\t"
    "buffer_load_dwordx4 %[w0], %[r2], %[t] offen lds\n\t"
    "s_add_u32 m0, m0, 1024\n\t"
    "s_nop 0\n\t"
    "buffer_load_dwordx4 %[w1], %[r2], %[t] offen lds\n\t"
    "v_mfma_f32_32x32x16_bf16 v[16:31], v[92:95], v[72:75], v[16:31]\n\t"
    "s_add_u32 %[t], %[hf], 10240\n\t"
    "buffer_load_dwordx4 v[64:67], %[h0], %[rs], %[t] offen sc1\n\t"
    "buffer_load_dwordx4 v[68:71], %[h1], %[rs], %[t] offen sc1\n\t"
    "s_mov_b64 exec, 0xff\n\t"
    "global_load_dwordx2 %[fv], %[fo], %[wsb] sc1\n\t"
    "s_mov_b64 exec, -1\n\t"
    "v_mfma_f32_32x32x16_bf16 v[48:63], v[92:95], v[76:79], v[48:63]\n\t"
    "s_waitcnt vmcnt(5) lgkmcnt(0)\n\t"
    "v_mfma_f32_32x32x16_bf16 v[0:15], v[96:99], v[80:83], v[0:15]\n\t"
    "ds_read_b64_tr_b16 v[88:89], %[b00] offset:49152\n\t"
    "ds_read_b64_tr_b16 v[90:91], %[b00] offset:49664\n\t"
    "ds_read_b64_tr_b16 v[92:93], %[b10] offset:49152\n\t"
    "ds_read_b64_tr_b16 v[94:95], %[b10] offset:49664\n\t"
    "v_mfma_f32_32x32x16_bf16 v[32:47], v[96:99], v[84:87], v[32:47]\n\t"
    "s_add_u32 m0, %[lb], 98304\n\t"
    "s_add_u32 %[t], %[wf], 57344\n\t"
    "buffer_load_dwordx4 %[w0], %[r2], %[t] offen lds\n\t"
    "s_add_u32 m0, m0, 1024\n\t"
    "s_nop 0\n\t"
    "buffer_load_dwordx4 %[w1], %[r2], %[t] offen lds\n\t"
    "v_mfma_f32_32x32x16_bf16 v[16:31], v[100:103], v[80:83], v[16:31]\n\t"
    "s_add_u32 %[t], %[hf], 11264\n\t"
    "buffer_load_dwordx4 v[72:75], %[h0], %[rs], %[t] offen sc1\n\t"
    "buffer_load_dwordx4 v[76:79], %[h1], %[rs], %[t] offen sc1\n\t"
    "v_mfma_f32_32x32x16_bf16 v[48:63], v[100:103], v[84:87], v[48:63]\n\t"
    "s_waitcnt vmcnt(5) lgkmcnt(0)\n\t"
    "v_mfma_f32_32x32x16_bf16 v[0:15], v[88:91], v[64:67], v[0:15]\n\t"
    "ds_read_b64_tr_b16 v[96:97], %[b01] offset:49152\n\t"
    "ds_read_b64_tr_b16 v[98:99], %[b01] offset:49664\n\t"
    "ds_read_b64_tr_b16 v[100:101], %[b11] offset:49152\n\t"
    "ds_read_b64_tr_b16 v[102:103], %[b11] offset:49664\n\t"
    "s_mov_b64 exec, 1\n\t"
    "ds_add_u32 %[ca], %[one]\n\t"
    "s_mov_b64 exec, -1\n\t"
    "v_mfma_f32_32x32x16_bf16 v[32:47], v[88:91], v[68:71], v[32:47]\n\t"
    "s_add_u32 m0, %[lb], 106496\n\t"
    "s_add_u32 %[t], %[wf], 71680\n\t"
    "buffer_load_dwordx4 %[w0], %[r2], %[t] offen lds\n\t"
    "s_add_u32 m0, m0, 1024\n\t"
    "s_nop 0\n\t"
    "buffer_load_dwordx4 %[w1], %[r2], %[t] offen lds\n\t"
    "v_mfma_f32_32x32x16_bf16 v[16:31], v[92:95], v[64:67], v[16:31]\n\t"
    "s_add_u32 %[t], %[hf], 16384\n\t"
    "buffer_load_dwordx4 v[80:83], %[h0], %[rs], %[t] offen sc1\n\t"
    "buffer_load_dwordx4 v[84:87], %[h1], %[rs], %[t] offen sc1\n\t"
    "v_mfma_f32_32x32x16_bf16 v[48:63], v[92:95], v[68:71], v[48:63]\n\t"
    "s_waitcnt vmcnt(4) lgkmcnt(0)\n\t"
    "v_mfma_f32_32x32x16_bf16 v[0:15], v[96:99], v[72:75], v[0:15]\n\t"
    "ds_read_b64_tr_b16 v[88:89], %[b01] offset:57344\n\t"
    "ds_read_b64_tr_b16 v[90:91], %[b01] offset:57856\n\t"
    "ds_read_b64_tr_b16 v[92:93], %[b11] offset:57344\n\t"
    "ds_read_b64_tr_b16 v[94:95], %[b11] offset:57856\n\t"
    "v_mfma_f32_32x32x16_bf16 v[32:47], v[96:99], v[76:79], v[32:47]\n\t"
    "s_add_u32 m0, %[lb], 114688\n\t"
    "s_add_u32 %[t], %[wf], 86016\n\t"
    "buffer_load_dwordx4 %[w0], %[r2], %[t] offen lds\n\t"
    "s_add_u32 m0, m0, 1024\n\t"
    "s_nop 0\n\t"
    "buffer_load_dwordx4 %[w1], %[r2], %[t] offen lds\n\t"
    "v_mfma_f32_32x32x16_bf16 v[16:31], v[100:103], v[72:75], v[16:31]\n\t"
    "s_add_u32 %[t], %[hf], 17408\n\t"
    "buffer_load_dwordx4 v[64:67], %[h0], %[rs], %[t] offen sc1\n\t"
    "buffer_load_dwordx4 v[68:71], %[h1], %[rs], %[t] offen sc1\n\t"
    "v_mfma_f32_32x32x16_bf16 v[48:63], v[100:103], v[76:79], v[48:63]\n\t"
    "s_waitcnt vmcnt(4) lgkmcnt(0)\n\t"
    "v_mfma_f32_32x32x16_bf16 v[0:15], v[88:91], v[80:83], v[0:15]\n\t"
    "ds_read_b64_tr_b16 v[96:97], %[b02] offset:0\n\t"
    "ds_read_b64_tr_b16 v[98:99], %[b02] offset:512\n\t"
    "ds_read_b64_tr_b16 v[100:101], %[b12] offset:0\n\t"
    "ds_read_b64_tr_b16 v[102:103], %[b12] offset:512\n\t"
    "v_mfma_f32_32x32x16_bf16 v[32:47], v[88:91], v[84:87], v[32:47]\n\t"
    "s_add_u32 m0, %[lb], 122880\n\t"
    "s_add_u32 %[t], %[wf], 100352\n\t"
    "buffer_load_dwordx4 %[w0], %[r2], %[t] offen lds\n\t"
    "s_add_u32 m0, m0, 1024\n\t"
    "s_nop 0\n\t"
    "buffer_load_dwordx4 %[w1], %[r2], %[t] offen lds\n\t"
    "v_mfma_f32_32x32x16_bf16 v[16:31], v[92:95], v[80:83], v[16:31]\n\t"
    "s_waitcnt vmcnt(14)\n\t"
    "s_setprio 3\n\t"
    "s_mov_b64 exec, 0xff\n\t"
    "s_mov_b32 %[t], 0x40000\n\t"
    "3:\n\t"
    "v_cmp_ne_u64 vcc, %[tg], %[fv]\n\t"
    "s_cbranch_vccz 4f\n\t"
    "s_sleep 4\n\t"
    "global_load_dwordx2 %[fv], %[fo], %[wsb] sc1\n\t"
    "s_sub_u32 %[t], %[t], 1\n\t"
    "s_waitcnt vmcnt(0)\n\t"
    "s_cbranch_scc0 3b\n\t"
    "4:\n\t"
    "s_mov_b64 exec, -1\n\t"
    "s_setprio %[pr]\n\t"
    "s_add_u32 %[t], %[hf], 18432\n\t"
    "buffer_load_dwordx4 v[72:75], %[h0], %[rs], %[t] offen sc1\n\t"
    "buffer_load_dwordx4 v[76:79], %[h1], %[rs], %[t] offen sc1\n\t"
    "v_mfma_f32_32x32x16_bf16 v[48:63], v[92:95], v[84:87], v[48:63]\n\t"
    "s_waitcnt vmcnt(4) lgkmcnt(0)\n\t"
    "v_mfma_f32_32x32x16_bf16 v[0:15], v[96:99], v[64:67], v[0:15]\n\t"
    "ds_read_b64_tr_b16 v[88:89], %[b02] offset:8192\n\t"
    "ds_read_b64_tr_b16 v[90:91], %[b02] offset:8704\n\t"
    "ds_read_b64_tr_b16 v[92:93], %[b12] offset:8192\n\t"
    "ds_read_b64_tr_b16 v[94:95], %[b12] offset:8704\n\t"
    "v_mfma_f32_32x32x16_bf16 v[32:47], v[96:99], v[68:71], v[32:47]\n\t"
    "s_add_u32 m0, %[lb], 131072\n\t"
    "s_add_u32 %[t], %[wf], 172032\n\t"
    "buffer_load_dwordx4 %[w0], %[r2], %[t] offen lds\n\t"
    "s_add_u32 m0, m0, 1024\n\t"
    "s_nop 0\n\t"
    "buffer_load_dwordx4 %[w1], %[r2], %[t] offen lds\n\t"
    "v_mfma_f32_32x32x16_bf16 v[16:31], v[100:103], v[64:67], v[16:31]\n\t"
    "s_add_u32 %[t], %[hf], 19456\n\t"
    "buffer_load_dwordx4 v[80:83], %[h0], %[rs], %[t] offen sc1\n\t"
    "buffer_load_dwordx4 v[84:87], %[h1], %[rs], %[t] offen sc1\n\t"
    "v_mfma_f32_32x32x16_bf16 v[48:63], v[100:103], v[68:71], v[48:63]\n\t"
    "s_waitcnt vmcnt(4) lgkmcnt(0)\n\t"
    "v_mfma_f32_32x32x16_bf16 v[0:15], v[88:91], v[72:75], v[0:15]\n\t"
    "ds_read_b64_tr_b16 v[96:97], %[b00] offset:57344\n\t"
    "ds_read_b64_tr_b16 v[98:99], %[b00] offset:57856\n\t"
    "ds_read_b64_tr_b16 v[100:101], %[b10] offset:57344\n\t"
    "ds_read_b64_tr_b16 v[102:103], %[b10] offset:57856\n\t"
    "v_mfma_f32_32x32x16_bf16 v[32:47], v[88:91], v[76:79], v[32:47]\n\t"
    "s_add_u32 m0, %[lb], 139264\n\t"
    "s_add_u32 %[t], %[wf], 186368\n\t"
    "buffer_load_dwordx4 %[w0], %[r2], %[t] offen lds\n\t"
    "s_add_u32 m0, m0, 1024\n\t"
    "s_nop 0\n\t"
    "buffer_load_dwordx4 %[w1], %[r2], %[t] offen lds\n\t"
    "v_mfma_f32_32x32x16_bf16 v[16:31], v[92:95], v[72:75], v[16:31]\n\t"
    "s_add_u32 %[t], %[hs], 0\n\t"
    "buffer_load_dwordx4 v[64:67], %[h0], %[rs], %[t] offen sc1\n\t"
    "buffer_load_dwordx4 v[68:71], %[h1], %[rs], %[t] offen sc1\n\t"
    "v_mfma_f32_32x32x16_bf16 v[48:63], v[92:95], v[76:79], v[48:63]\n\t"
    "s_waitcnt vmcnt(4) lgkmcnt(0)\n\t"
    "v_mfma_f32_32x32x16_bf16 v[0:15], v[96:99], v[80:83], v[0:15]\n\t"
    "ds_read_b64_tr_b16 v[88:89], %[b01] offset:0\n\t"
    "ds_read_b64_tr_b16 v[90:91], %[b01] offset:512\n\t"
    "ds_read_b64_tr_b16 v[92:93], %[b11] offset:0\n\t"
    "ds_read_b64_tr_b16 v[94:95], %[b11] offset:512\n\t"
    "v_mfma_f32_32x32x16_bf16 v[32:47], v[96:99], v[84:87], v[32:47]\n\t"
    "s_add_u32 m0, %[lb], 57344\n\t"
    "s_add_u32 %[t], %[wf], 200704\n\t"
    "buffer_load_dwordx4 %[w0], %[r2], %[t] offen lds\n\t"
    "s_add_u32 m0, m0, 1024\n\t"
    "s_nop 0\n\t"
    "buffer_load_dwordx4 %[w1], %[r2], %[t] offen lds\n\t"
    "v_mfma_f32_32x32x16_bf16 v[16:31], v[100:103], v[80:83], v[16:31]\n\t"
    "s_add_u32 %[t], %[hs], 1024\n\t"
    "buffer_load_dwordx4 v[72:75], %[h0], %[rs], %[t] offen sc1\n\t"
    "buffer_load_dwordx4 v[76:79], %[h1], %[rs], %[t] offen sc1\n\t"
    "v_mfma_f32_32x32x16_bf16 v[48:63], v[100:103], v[84:87], v[48:63]\n\t"
    "s_waitcnt vmcnt(4) lgkmcnt(0)\n\t"
    "v_mfma_f32_32x32x16_bf16 v[0:15], v[88:91], v[64:67], v[0:15]\n\t"
    "ds_read_b64_tr_b16 v[96:97], %[b01] offset:8192\n\t"
    "ds_read_b64_tr_b16 v[98:99], %[b01] offset:8704\n\t"
    "ds_read_b64_tr_b16 v[100:101], %[b11] offset:8192\n\t"
    "ds_read_b64_tr_b16 v[102:103], %[b11] offset:8704\n\t"
    "v_mfma_f32_32x32x16_bf16 v[32:47], v[88:91], v[68:71], v[32:47]\n\t"
    "s_add_u32 m0, %[lb], 65536\n\t"
    "s_add_u32 %[t], %[wf], 215040\n\t"
    "buffer_load_dwordx4 %[w0], %[r2], %[t] offen lds\n\t"
    "s_add_u32 m0, m0, 1024\n\t"
    "s_nop 0\n\t"
    "buffer_load_dwordx4 %[w1], %[r2], %[t] offen lds\n\t"
    "v_mfma_f32_32x32x16_bf16 v[16:31], v[92:95], v[64:67], v[16:31]\n\t"
    "s_add_u32 %[t], %[hs], 2048\n\t"
    "buffer_load_dwordx4 v[80:83], %[h0], %[rs], %[t] offen sc1\n\t"
    "buffer_load_dwordx4 v[84:87], %[h1], %[rs], %[t] offen sc1\n\t"
    "v_mfma_f32_32x32x16_bf16 v[48:63], v[92:95], v[68:71], v[48:63]\n\t"
    "s_waitcnt vmcnt(4) lgkmcnt(0)\n\t"
    "v_mfma_f32_32x32x16_bf16 v[0:15], v[96:99], v[72:75], v[0:15]\n\t"
    "ds_read_b64_tr_b16 v[88:89], %[b01] offset:16384\n\t"
    "ds_read_b64_tr_b16 v[90:91], %[b01] offset:16896\n\t"
    "ds_read_b64_tr_b16 v[92:93], %[b11] offset:16384\n\t"
    "ds_read_b64_tr_b16 v[94:95], %[b11] offset:16896\n\t"
    "v_mfma_f32_32x32x16_bf16 v[32:47], v[96:99], v[76:79], v[32:47]\n\t"
    "s_add_u32 m0, %[lb], 73728\n\t"
    "s_add_u32 %[t], %[wf], 286720\n\t"
    "buffer_load_dwordx4 %[w0], %[r2], %[t] offen lds\n\t"
    "s_add_u32 m0, m0, 1024\n\t"
    "s_nop 0\n\t"
    "buffer_load_dwordx4 %[w1], %[r2], %[t] offen lds\n\t"
    "v_mfma_f32_32x32x16_bf16 v[16:31], v[100:103], v[72:75], v[16:31]\n\t"
    "s_add_u32 %[t], %[hs], 3072\n\t"
    "buffer_load_dwordx4 v[64:67], %[h0], %[rs], %[t] offen sc1\n\t"
    "buffer_load_dwordx4 v[68:71], %[h1], %[rs], %[t] offen sc1\n\t"
    "v_mfma_f32_32x32x16_bf16 v[48:63], v[100:103], v[76:79], v[48:63]\n\t"
    "s_waitcnt vmcnt(4) lgkmcnt(0)\n\t"
    "v_mfma_f32_32x32x16_bf16 v[0:15], v[88:91], v[80:83], v[0:15]\n\t"
    "ds_read_b64_tr_b16 v[96:97], %[b01] offset:24576\n\t"
    "ds_read_b64_tr_b16 v[98:99], %[b01] offset:25088\n\t"
    "ds_read_b64_tr_b16 v[100:101], %[b11] offset:24576\n\t"
    "ds_read_b64_tr_b16 v[102:103], %[b11] offset:25088\n\t"
    "v_mfma_f32_32x32x16_bf16 v[32:47], v[88:91], v[84:87], v[32:47]\n\t"
    "s_add_u32 m0, %[lb], 81920\n\t"
    "s_add_u32 %[t], %[wf], 301056\n\t"
    "buffer_load_dwordx4 %[w0], %[r2], %[t] offen lds\n\t"
    "s_add_u32 m0, m0, 1024\n\t"
    "s_nop 0\n\t"
    "buffer_load_dwordx4 %[w1], %[r2], %[t] offen lds\n\t"
    "v_mfma_f32_32x32x16_bf16 v[16:31], v[92:95], v[80:83], v[16:31]\n\t"
    "s_add_u32 %[t], %[hf], 4096\n\t"
    "buffer_load_dwordx4 v[72:75], %[h0], %[rs], %[t] offen sc1\n\t"
    "buffer_load_dwordx4 v[76:79], %[h1], %[rs], %[t] offen sc1\n\t"
    "v_mfma_f32_32x32x16_bf16 v[48:63], v[92:95], v[84:87], v[48:63]\n\t"
    "s_waitcnt vmcnt(4) lgkmcnt(0)\n\t"
    "v_mfma_f32_32x32x16_bf16 v[0:15], v[96:99], v[64:67], v[0:15]\n\t"
    "ds_read_b64_tr_b16 v[88:89], %[b01] offset:32768\n\t"
    "ds_read_b64_tr_b16 v[90:91], %[b01] offset:33280\n\t"
    "ds_read_b64_tr_b16 v[92:93], %[b11] offset:32768\n\t"
    "ds_read_b64_tr_b16 v[94:95], %[b11] offset:33280\n\t"
    "v_mfma_f32_32x32x16_bf16 v[32:47], v[96:99], v[68:71], v[32:47]\n\t"
    "s_add_u32 m0, %[lb], 90112\n\t"
    "s_add_u32 %[t], %[wf], 315392\n\t"
    "buffer_load_dwordx4 %[w0], %[r2], %[t] offen lds\n\t"
    "s_add_u32 m0, m0, 1024\n\t"
    "s_nop 0\n\t"
    "buffer_load_dwordx4 %[w1], %[r2], %[t] offen lds\n\t"
    "v_mfma_f32_32x32x16_bf16 v[16:31], v[100:103], v[64:67], v[16:31]\n\t"
    "s_add_u32 %[t], %[hf], 5120\n\t"
    "buffer_load_dwordx4 v[80:83], %[h0], %[rs], %[t] offen sc1\n\t"
    "buffer_load_dwordx4 v[84:87], %[h1], %[rs], %[t] offen sc1\n\t"
    "v_mfma_f32_32x32x16_bf16 v[48:63], v[100:103], v[68:71], v[48:63]\n\t"
    "s_waitcnt vmcnt(4) lgkmcnt(0)\n\t"
    "v_mfma_f32_32x32x16_bf16 v[0:15], v[88:91], v[72:75], v[0:15]\n\t"
    "ds_read_b64_tr_b16 v[96:97], %[b01] offset:40960\n\t"
    "ds_read_b64_tr_b16 v[98:99], %[b01] offset:41472\n\t"
    "ds_read_b64_tr_b16 v[100:101], %[b11] offset:40960\n\t"
    "ds_read_b64_tr_b16 v[102:103], %[b11] offset:41472\n\t"
    "v_mfma_f32_32x32x16_bf16 v[32:47], v[88:91], v[76:79], v[32:47]\n\t"
    "s_add_u32 m0, %[lb], 98304\n\t"
    "s_add_u32 %[t], %[wf], 329728\n\t"
    "buffer_load_dwordx4 %[w0], %[r2], %[t] offen lds\n\t"
    "s_add_u32 m0, m0, 1024\n\t"
    "s_nop 0\n\t"
    "buffer_load_dwordx4 %[w1], %[r2], %[t] offen lds\n\t"
    "v_mfma_f32_32x32x16_bf16 v[16:31], v[92:95], v[72:75], v[16:31]\n\t"
    "s_add_u32 %[t], %[hf], 6144\n\t"
    "buffer_load_dwordx4 v[64:67], %[h0], %[rs], %[t] offen sc1\n\t"
    "buffer_load_dwordx4 v[68:71], %[h1], %[rs], %[t] offen sc1\n\t"
    "v_mfma_f32_32x32x16_bf16 v[48:63], v[92:95], v[76:79], v[48:63]\n\t"
    "s_waitcnt vmcnt(4) lgkmcnt(0)\n\t"
    "v_mfma_f32_32x32x16_bf16 v[0:15], v[96:99], v[80:83], v[0:15]\n\t"
    "ds_read_b64_tr_b16 v[88:89], %[b01] offset:49152\n\t"
    "ds_read_b64_tr_b16 v[90:91], %[b01] offset:49664\n\t"
    "ds_read_b64_tr_b16 v[92:93], %[b11] offset:49152\n\t"
    "ds_read_b64_tr_b16 v[94:95], %[b11] offset:49664\n\t"
    "v_mfma_f32_32x32x16_bf16 v[32:47], v[96:99], v[84:87], v[32:47]\n\t"
    "s_add_u32 m0, %[lb], 106496\n\t"
    "s_add_u32 %[t], %[wo], 0\n\t"
    "buffer_load_dwordx4 %[w0], %[ro], %[t] offen lds\n\t"
    "s_add_u32 m0, m0, 1024\n\t"
    "s_nop 0\n\t"
    "buffer_load_dwordx4 %[w1], %[ro], %[t] offen lds\n\t"
    "v_mfma_f32_32x32x16_bf16 v[16:31], v[100:103], v[80:83], v[16:31]\n\t"
    "s_add_u32 %[t], %[hf], 7168\n\t"
    "buffer_load_dwordx4 v[72:75], %[h0], %[rs], %[t] offen sc1\n\t"
    "buffer_load_dwordx4 v[76:79], %[h1], %[rs], %[t] offen sc1\n\t"
    "v_mfma_f32_32x32x16_bf16 v[48:63], v[100:103], v[84:87], v[48:63]\n\t"
    "s_waitcnt vmcnt(4) lgkmcnt(0)\n\t"
    "v_mfma_f32_32x32x16_bf16 v[0:15], v[88:91], v[64:67], v[0:15]\n\t"
    "ds_read_b64_tr_b16 v[96:97], %[b01] offset:57344\n\t"
    "ds_read_b64_tr_b16 v[98:99], %[b01] offset:57856\n\t"
    "ds_read_b64_tr_b16 v[100:101], %[b11] offset:57344\n\t"
    "ds_read_b64_tr_b16 v[102:103], %[b11] offset:57856\n\t"
    "v_mfma_f32_32x32x16_bf16 v[32:47], v[88:91], v[68:71], v[32:47]\n\t"
    "s_add_u32 m0, %[lb], 114688\n\t"
    "s_add_u32 %[t], %[wo], 14336\n\t"
    "buffer_load_dwordx4 %[w0], %[ro], %[t] offen lds\n\t"
    "s_add_u32 m0, m0, 1024\n\t"
    "s_nop 0\n\t"
    "buffer_load_dwordx4 %[w1], %[ro], %[t] offen lds\n\t"
    "v_mfma_f32_32x32x16_bf16 v[16:31], v[92:95], v[64:67], v[16:31]\n\t"
    "s_add_u32 %[t], %[hf], 12288\n\t"
    "buffer_load_dwordx4 v[80:83], %[h0], %[rs], %[t] offen sc1\n\t"
    "buffer_load_dwordx4 v[84:87], %[h1], %[rs], %[t] offen sc1\n\t"
    "v_mfma_f32_32x32x16_bf16 v[48:63], v[92:95], v[68:71], v[48:63]\n\t"
    "s_waitcnt vmcnt(4) lgkmcnt(0)\n\t"
    "v_mfma_f32_32x32x16_bf16 v[0:15], v[96:99], v[72:75], v[0:15]\n\t"
    "ds_read_b64_tr_b16 v[88:89], %[b02] offset:0\n\t"
    "ds_read_b64_tr_b16 v[90:91], %[b02] offset:512\n\t"
    "ds_read_b64_tr_b16 v[92:93], %[b12] offset:0\n\t"
    "ds_read_b64_tr_b16 v[94:95], %[b12] offset:512\n\t"
    "v_mfma_f32_32x32x16_bf16 v[32:47], v[96:99], v[76:79], v[32:47]\n\t"
    "s_add_u32 m0, %[lb], 122880\n\t"
    "s_add_u32 %[t], %[wo], 28672\n\t"
    "buffer_load_dwordx4 %[w0], %[ro], %[t] offen lds\n\t"
    "s_add_u32 m0, m0, 1024\n\t"
    "s_nop 0\n\t"
    "buffer_load_dwordx4 %[w1], %[ro], %[t] offen lds\n\t"
    "v_mfma_f32_32x32x16_bf16 v[16:31], v[100:103], v[72:75], v[16:31]\n\t"
    "s_add_u32 %[t], %[hf], 13312\n\t"
    "buffer_load_dwordx4 v[64:67], %[h0], %[rs], %[t] offen sc1\n\t"
    "buffer_load_dwordx4 v[68:71], %[h1], %[rs], %[t] offen sc1\n\t"
    "v_mfma_f32_32x32x16_bf16 v[48:63], v[100:103], v[76:79], v[48:63]\n\t"
    "s_waitcnt vmcnt(4) lgkmcnt(0)\n\t"
    "v_mfma_f32_32x32x16_bf16 v[0:15], v[88:91], v[80:83], v[0:15]\n\t"
    "ds_read_b64_tr_b16 v[96:97], %[b02] offset:8192\n\t"
    "ds_read_b64_tr_b16 v[98:99], %[b02] offset:8704\n\t"
    "ds_read_b64_tr_b16 v[100:101], %[b12] offset:8192\n\t"
    "ds_read_b64_tr_b16 v[102:103], %[b12] offset:8704\n\t"
    "v_mfma_f32_32x32x16_bf16 v[32:47], v[88:91], v[84:87], v[32:47]\n\t"
    "s_add_u32 m0, %[lb], 131072\n\t"
    "s_add_u32 %[t], %[wo], 43008\n\t"
    "buffer_load_dwordx4 %[w0], %[ro], %[t] offen lds\n\t"
    "s_add_u32 m0, m0, 1024\n\t"
    "s_nop 0\n\t"
    "buffer_load_dwordx4 %[w1], %[ro], %[t] offen lds\n\t"
    "v_mfma_f32_32x32x16_bf16 v[16:31], v[92:95], v[80:83], v[16:31]\n\t"
    "s_add_u32 %[t], %[hf], 14336\n\t"
    "buffer_load_dwordx4 v[72:75], %[h0], %[rs], %[t] offen sc1\n\t"
    "buffer_load_dwordx4 v[76:79], %[h1], %[rs], %[t] offen sc1\n\t"
    "v_mfma_f32_32x32x16_bf16 v[48:63], v[92:95], v[84:87], v[48:63]\n\t"
    "s_waitcnt vmcnt(4) lgkmcnt(0)\n\t"
    "v_mfma_f32_32x32x16_bf16 v[0:15], v[96:99], v[64:67], v[0:15]\n\t"
    "ds_read_b64_tr_b16 v[88:89], %[b00] offset:57344\n\t"
    "ds_read_b64_tr_b16 v[90:91], %[b00] offset:57856\n\t"
    "ds_read_b64_tr_b16 v[92:93], %[b10] offset:57344\n\t"
    "ds_read_b64_tr_b16 v[94:95], %[b10] offset:57856\n\t"
    "v_mfma_f32_32x32x16_bf16 v[32:47], v[96:99], v[68:71], v[32:47]\n\t"
    "s_add_u32 m0, %[lb], 139264\n\t"
    "s_add_u32 %[t], %[wo], 57344\n\t"
    "buffer_load_dwordx4 %[w0], %[ro], %[t] offen lds\n\t"
    "s_add_u32 m0, m0, 1024\n\t"
    "s_nop 0\n\t"
    "buffer_load_dwordx4 %[w1], %[ro], %[t] offen lds\n\t"
    "v_mfma_f32_32x32x16_bf16 v[16:31], v[100:103], v[64:67], v[16:31]\n\t"
    "s_add_u32 %[t], %[hf], 15360\n\t"
    "buffer_load_dwordx4 v[80:83], %[h0], %[rs], %[t] offen sc1\n\t"
    "buffer_load_dwordx4 v[84:87], %[h1], %[rs], %[t] offen sc1\n\t"
    "v_mfma_f32_32x32x16_bf16 v[48:63], v[100:103], v[68:71], v[48:63]\n\t"
    "s_waitcnt vmcnt(4) lgkmcnt(0)\n\t"
    "v_mfma_f32_32x32x16_bf16 v[0:15], v[88:91], v[72:75], v[0:15]\n\t"
    "ds_read_b64_tr_b16 v[96:97], %[b01] offset:0\n\t"
    "ds_read_b64_tr_b16 v[98:99], %[b01] offset:512\n\t"
    "ds_read_b64_tr_b16 v[100:101], %[b11] offset:0\n\t"
    "ds_read_b64_tr_b16 v[102:103], %[b11] offset:512\n\t"
    "v_mfma_f32_32x32x16_bf16 v[32:47], v[88:91], v[76:79], v[32:47]\n\t"
    "s_add_u32 m0, %[lb], 57344\n\t"
    "s_add_u32 %[t], %[wo], 71680\n\t"
    "buffer_load_dwordx4 %[w0], %[ro], %[t] offen lds\n\t"
    "s_add_u32 m0, m0, 1024\n\t"
    "s_nop 0\n\t"
    "buffer_load_dwordx4 %[w1], %[ro], %[t] offen lds\n\t"
    "v_mfma_f32_32x32x16_bf16 v[16:31], v[92:95], v[72:75], v[16:31]\n\t"
    "s_add_u32 %[t], %[hf], 20480\n\t"
    "buffer_load_dwordx4 v[64:67], %[h0], %[rs], %[t] offen sc1\n\t"
    "buffer_load_dwordx4 v[68:71], %[h1], %[rs], %[t] offen sc1\n\t"
    "v_mfma_f32_32x32x16_bf16 v[48:63], v[92:95], v[76:79], v[48:63]\n\t"
    "s_waitcnt vmcnt(4) lgkmcnt(0)\n\t"
    "v_mfma_f32_32x32x16_bf16 v[0:15], v[96:99], v[80:83], v[0:15]\n\t"
    "ds_read_b64_tr_b16 v[88:89], %[b01] offset:8192\n\t"
    "ds_read_b64_tr_b16 v[90:91], %[b01] offset:8704\n\t"
    "ds_read_b64_tr_b16 v[92:93], %[b11] offset:8192\n\t"
    "ds_read_b64_tr_b16 v[94:95], %[b11] offset:8704\n\t"
    "v_mfma_f32_32x32x16_bf16 v[32:47], v[96:99], v[84:87], v[32:47]\n\t"
    "s_add_u32 m0, %[lb], 65536\n\t"
    "s_add_u32 %[t], %[wo], 86016\n\t"
    "buffer_load_dwordx4 %[w0], %[ro], %[t] offen lds\n\t"
    "s_add_u32 m0, m0, 1024\n\t"
    "s_nop 0\n\t"
    "buffer_load_dwordx4 %[w1], %[ro], %[t] offen lds\n\t"
    "v_mfma_f32_32x32x16_bf16 v[16:31], v[100:103], v[80:83], v[16:31]\n\t"
    "s_add_u32 %[t], %[hf], 21504\n\t"
    "buffer_load_dwordx4 v[72:75], %[h0], %[rs], %[t] offen sc1\n\t"
    "buffer_load_dwordx4 v[76:79], %[h1], %[rs], %[t] offen sc1\n\t"
    "v_mfma_f32_32x32x16_bf16 v[48:63], v[100:103], v[84:87], v[48:63]\n\t"
    "s_waitcnt vmcnt(4) lgkmcnt(0)\n\t"
    "v_mfma_f32_32x32x16_bf16 v[0:15], v[88:91], v[64:67], v[0:15]\n\t"
    "ds_read_b64_tr_b16 v[96:97], %[b01] offset:16384\n\t"
    "ds_read_b64_tr_b16 v[98:99], %[b01] offset:16896\n\t"
    "ds_read_b64_tr_b16 v[100:101], %[b11] offset:16384\n\t"
    "ds_read_b64_tr_b16 v[102:103], %[b11] offset:16896\n\t"
    "v_mfma_f32_32x32x16_bf16 v[32:47], v[88:91], v[68:71], v[32:47]\n\t"
    "v_mfma_f32_32x32x16_bf16 v[16:31], v[92:95], v[64:67], v[16:31]\n\t"
    "s_add_u32 %[t], %[hf], 22528\n\t"
    "buffer_load_dwordx4 v[80:83], %[h0], %[rs], %[t] offen sc1\n\t"
    "buffer_load_dwordx4 v[84:87], %[h1], %[rs], %[t] offen sc1\n\t"
    "v_mfma_f32_32x32x16_bf16 v[48:63], v[92:95], v[68:71], v[48:63]\n\t"
    "s_waitcnt vmcnt(2) lgkmcnt(0)\n\t"
    "v_mfma_f32_32x32x16_bf16 v[0:15], v[96:99], v[72:75], v[0:15]\n\t"
    "ds_read_b64_tr_b16 v[88:89], %[b01] offset:24576\n\t"
    "ds_read_b64_tr_b16 v[90:91], %[b01] offset:25088\n\t"
    "ds_read_b64_tr_b16 v[92:93], %[b11] offset:24576\n\t"
    "ds_read_b64_tr_b16 v[94:95], %[b11] offset:25088\n\t"
    "v_mfma_f32_32x32x16_bf16 v[32:47], v[96:99], v[76:79], v[32:47]\n\t"
    "v_mfma_f32_32x32x16_bf16 v[16:31], v[100:103], v[72:75], v[16:31]\n\t"
    "s_add_u32 %[t], %[hf], 23552\n\t"
    "buffer_load_dwordx4 v[64:67], %[h0], %[rs], %[t] offen sc1\n\t"
    "buffer_load_dwordx4 v[68:71], %[h1], %[rs], %[t] offen sc1\n\t"
    "v_mfma_f32_32x32x16_bf16 v[48:63], v[100:103], v[76:79], v[48:63]\n\t"
    "s_waitcnt vmcnt(2) lgkmcnt(0)\n\t"
    "v_mfma_f32_32x32x16_bf16 v[0:15], v[88:91], v[80:83], v[0:15]\n\t"
    "ds_read_b64_tr_b16 v[96:97], %[b01] offset:32768\n\t"
    "ds_read_b64_tr_b16 v[98:99], %[b01] offset:33280\n\t"
    "ds_read_b64_tr_b16 v[100:101], %[b11] offset:32768\n\t"
    "ds_read_b64_tr_b16 v[102:103], %[b11] offset:33280\n\t"
    "v_mfma_f32_32x32x16_bf16 v[32:47], v[88:91], v[84:87], v[32:47]\n\t"
    "v_mfma_f32_32x32x16_bf16 v[16:31], v[92:95], v[80:83], v[16:31]\n\t"
    "v_mfma_f32_32x32x16_bf16 v[48:63], v[92:95], v[84:87], v[48:63]\n\t"
    "s_waitcnt vmcnt(0) lgkmcnt(0)\n\t"
    "v_mfma_f32_32x32x16_bf16 v[0:15], v[96:99], v[64:67], v[0:15]\n\t"
    "v_mfma_f32_32x32x16_bf16 v[32:47], v[96:99], v[68:71], v[32:47]\n\t"
    "v_mfma_f32_32x32x16_bf16 v[16:31], v[100:103], v[64:67], v[16:31]\n\t"
    "v_mfma_f32_32x32x16_bf16 v[48:63], v[100:103], v[68:71], v[48:63]\n\t"
    "s_mov_b32 m0, %[sv]\n\t"
    "s_setprio 2\n\t"
    "s_nop 11\n\t"
    : "=&{v[0:15]}"(c[0]), "=&{v[16:31]}"(c[1]), "=&{v[32:47]}"(c[2]), "=&{v[48:63]}"(c[3]), [sv] "=&s"(sv), [t] "=&s"(t), [fv] "=&v"(fv), [tv] "=&v"(tv)
    : [b00] "v"(wb.b[0][0]), [b01] "v"(wb.b[0][1]), [b02] "v"(wb.b[0][2]), [b10] "v"(wb.b[1][0]), [b11] "v"(wb.b[1][1]), [b12] "v"(wb.b[1][2]),
      [h0] "v"(A.h0), [h1] "v"(A.h1), [w0] "v"(A.w0), [w1] "v"(A.w0 + 8u * DM * 2u), [ca] "v"(A.ca), [one] "v"(1u), [fo] "v"(A.fo),
      [rs] "s"(A.rs), [r2] "s"(A.r2), [ro] "s"(A.ro), [hf] "s"(A.hf), [hs] "s"(A.hs), [wf] "s"(A.wf), [ws] "s"(A.wsp), [wo] "s"(A.wo), [lb] "s"(A.lb),
      [tg] "s"(A.tag), [wsb] "s"(A.ws), [pr] "n"(0)
    : "scc", "vcc", "memory", "v64", "v65", "v66", "v67", "v68", "v69", "v70", "v71", "v72", "v73", "v74", "v75", "v76", "v77", "v78", "v79", "v80", "v81", "v82", "v83", "v84", "v85", "v86", "v87", "v88", "v89", "v90", "v91", "v92", "v93", "v94", "v95", "v96", "v97", "v98", "v99", "v100", "v101", "v102", "v103", "v104", "v105", "v106", "v107", "v108", "v109", "v110", "v111", "v112", "v113", "v114", "v115", "v116", "v117", "v118", "v119", "v120", "v121", "v122", "v123", "v124", "v125", "v126", "v127", "v128", "v129", "v130", "v131", "v132", "v133", "v134", "v135", "v136", "v137", "v138", "v139", "v140", "v141", "v142", "v143");
}

DI void f_wave(const Ptrs& P, rsrc_t rs, char* lds, int h, int j, int t, int rt, int ct, int fw) {
    char* ws = P.ws;
    const unsigned l0 = (unsigned)(uintptr_t)lds;
    __builtin_amdgcn_s_setprio(2);
    u32x16 xv[7];
    {
        const int lane = lane_id();
        if (fw == 0 && lane < 16) *((LAS unsigned*)((lds_ptr)lds + L_CTL) + lane) = 0u;
        if (j == 14 && lane < 14) glds16s(lane * 16, P.gw + fw * 112, l0 + L_GATE + fw * 224);
        wait_vm<0>();
        x_only(xv, lds, fw);
        lds_arrive(lds, C_X6);
    }
    const u64 tag = make_tag(lds);
    const int rowblk = t * 4 + fw;
    const unsigned myflag = WS_FH + ((t * 2 + (fw >> 1)) * 16 + j) * FL + (fw & 1) * 8;
    const int ctc = ct < 7 ? ct : 0;
    const int jf = 7 * (fw >> 1) + (fw & 1) * 4, js = 7 * (fw >> 1) + 3, kf = jf * 8, ksp = js * 8 + (fw & 1) * 4;
    const u16* wsrc = P.w2 + (size_t)(16 * kf) * DM + 64 * ctc;
    auto w2_voff = [](int lane) { return (unsigned)((lane >> 3) * (DM * 2) + ((lane & 7) ^ (((lane >> 4) & 1) << 2)) * 16); };
    if (j < 14) {
        lds_wait(lds, C_W1A, 4);
        const int lane = lane_id();
        ffn_up(xv, l0 + w_lane_off(lane, true), lane * 16, srd(ws, WS_END), WS_H + (unsigned)(rowblk * 112 + j * 8) * 1024u);
    } else if (j == 14) {
        const int lane = lane_id();
        f32x16 c = {};
#pragma unroll
        for (int ks = 0; ks < 28; ++ks) c = MFMA(lds128((lds_cptr)lds + L_GATE + ks * 32 + (lane >> 5) * 16), part4(xv[ks >> 2], ks & 3), c);
        if (lane < 32) __hip_atomic_store((gu64*)(ws + WS_G) + rowblk * 32 + lane, gate_word(sigmoidf_(c[0]), tag), RLX_AGENT);
    }
    __builtin_amdgcn_s_setprio(3);
    lds_wait(lds, C_WQ, 4);
    {
        f32x16 q;
        const int l = lane_id(), p4 = l & 3;
        q_proj(xv, q, l0 + L_RING + (8 * (l >> 5) + ((l & 15) >> 2)) * 64 + ((l >> 4) & 1) * 32 + (((p4 & 1) << 1) | (p4 >> 1)) * 8);
        q_store(q, lds, h, fw);
    }
    if (j < 14) publish(ws, myflag, tag);
    const int lane = lane_id(), r = lane & 31, hh = lane >> 5, wlo = w_lane_off(lane, true);
    const unsigned wo_ = w2_voff(lane);
    lds_wait(lds, C_Q, 4);
    if (ct >= 7) {
        lds_arrive(lds, C_MFREE);
        return;
    }
#pragma unroll 1
    for (int i = 0; i < 11; ++i) {
        const unsigned sl = l0 + (i < 7 ? i : i + 7) * 8192 + fw * 2048;
        const u16* s = wsrc + (size_t)((i >> 2) * 8 + (i & 3)) * 16 * DM;
        glds16s(wo_, s, sl); glds16s(wo_, s + 8 * DM, sl + 1024);
        wait_vm<6>();
    }
    poll_at(ws, WS_FH + (rt * 16 + jf + ((lane >> 1) & 3)) * FL + (lane & 1) * 8, 6, tag);
    wait_vm<0>();
    lds_wait(lds, C_FFN1, 4);
    const unsigned fo2 = WS_FH + (rt * 16 + ((lane & 7) < 6 ? jf + ((lane & 7) >> 1) : js)) * FL + (lane & 1) * 8 + ((lane & 7) < 6 || (fw & 1) ? 16 : 0);
    f32x16 c[4];
    WBase wb;
#pragma unroll
    for (int i = 0; i < 6; ++i) { wb.b[i & 1][i >> 1] = l0 + fw * 2048 + (wlo ^ (((i ^ fw) & 1) * 64)) + (i >> 1) * 65536; PIN(wb.b[i & 1][i >> 1]); }
    const int g0row = rt * 64 + (fw >> 1) * 32 + r, g1row = g0row ^ 32;
    const u64 gv0 = gate_load(ws, g0row), gv1 = gate_load(ws, g1row);
    float g0 = 1.f - gate_get(ws, g0row, gv0, tag), g1 = 1.f - gate_get(ws, g1row, gv1, tag);
    if (blockIdx.x == 0 && fw == 0) poll_at(ws, WS_FKV + (224 + (lane & 31)) * FL, 32, tag);
    PIN(g0); PIN(g1);
    {
        Ffn2Args A;
        A.rs = srd(ws, WS_END); A.r2 = srd(P.w2, FF * DM * 2); A.ro = srd(P.wo, DM * DM * 2);
        A.hf = WS_H + (rt * 224 + kf) * 1024; A.hs = WS_H + (rt * 224 + ksp) * 1024;
        A.wf = kf * 14336 + ct * 128; A.wsp = ksp * 14336 + ct * 128; A.wo = fw * 7 * 14336 + ct * 128;
        A.lb = l0 + fw * 2048; A.h0 = lane * 16 + (fw >> 1) * 114688; A.h1 = lane * 16 + ((fw >> 1) ^ 1) * 114688; A.w0 = wo_; A.ca = l0 + L_CTL + C_MFREE * 4; A.fo = fo2; A.tag = tag; A.ws = ws;
        ffn_down(c, wb, A);
    }
#pragma unroll
    for (int i = 0; i < 16; ++i) { c[0][i] *= g0; c[1][i] *= g0; c[2][i] *= g1; c[3][i] *= g1; }
    PIN(c[0]); PIN(c[1]); PIN(c[2]); PIN(c[3]);
    const int l2 = lane_id();
    unsigned ob0 = WS_O + (unsigned)(rt * 2 + (fw >> 1)) * 28672u + fw * 7168u, ob1 = WS_O + (unsigned)(rt * 2 + ((fw >> 1) ^ 1)) * 28672u + fw * 7168u;
    unsigned pw = l0 + 7 * 8192 + fw * 2048 + l2 * 16, vo = l2 * 16;
    u16* od = P.out + (size_t)(rt * 64 + (fw >> 1) * 32 + (l2 & 31)) * DM + 64 * ct + (fw & 1) * 32 + 8 * (l2 >> 5);
    wait_vm<0>();
    bf16x8 wa[4][2];
#pragma unroll
    for (int i = 0; i < 4; ++i) { wa[i][0] = wb.frag(28 + i, 0); wa[i][1] = wb.frag(28 + i, 1); }
    asm volatile("" : "+s"(ob0), "+s"(ob1));
    PIN(pw); PIN(od); PIN(vo);
    poll_at(ws, WS_FO + (rt * 16 + 4 * fw + ((l2 >> 1) & 3)) * FL + (l2 & 1) * 8, 8, tag);
    {
        bf16x8 of[7][2];
#pragma unroll
        for (int i = 0; i < 7; ++i) { of[i][0] = ld16(rs, vo, ob0 + i * 1024); of[i][1] = ld16(rs, vo, ob1 + i * 1024); }
        SBAR();
#pragma unroll
        for (int i = 0; i < 7; ++i) {
            c[3] = MFMA(wa[i & 3][1], of[i][1], c[3]); c[2] = MFMA(wa[i & 3][0], of[i][1], c[2]);
            c[1] = MFMA(wa[i & 3][1], of[i][0], c[1]); c[0] = MFMA(wa[i & 3][0], of[i][0], c[0]);
            PIN(c[0]); SBAR();
            if (i < 3) { wa[i][0] = wb.frag(32 + i, 0); wa[i][1] = wb.frag(32 + i, 1); SBAR(); }
        }
    }
#pragma unroll
    for (int i = 3; i > 0; --i)
#pragma unroll
        for (int g = 0; g < 4; ++g)
            *(LAS f32x4*)(uintptr_t)(pw + (i * 2 + (g >> 1)) * 8192 + (g & 1) * 1024) = f32x4{c[i][4 * g], c[i][4 * g + 1], c[i][4 * g + 2], c[i][4 * g + 3]};
    bar_lds();
    f32x4 pv[12];
#pragma unroll
    for (int i = 1; i < 4; ++i)
#pragma unroll
        for (int g = 0; g < 4; ++g) pv[(i - 1) * 4 + g] = *(const LAS f32x4*)(uintptr_t)((pw ^ (i * 2048)) + (i * 2 + (g >> 1)) * 8192 + (g & 1) * 1024);
    SBAR();
#pragma unroll
    for (int e = 0; e < 16; ++e) c[0][e] = (c[0][e] + pv[e >> 2][e & 3]) + (pv[4 + (e >> 2)][e & 3] + pv[8 + (e >> 2)][e & 3]);
    *(GAS u32x4*)od = pack8u(c[0], 0); *(GAS u32x4*)(od + 16) = pack8u(c[0], 1);
    if (blockIdx.x == 0 && fw == 0 && l2 == 0) __hip_atomic_store((gu64*)(ws + WS_EP), tag, RLX_AGENT);
}

__global__ __launch_bounds__(768, 3) void fused_block(Ptrs P) {
    extern __shared__ __attribute__((aligned(16))) char lds[];
    const int xcd = blockIdx.x & 7, idx = blockIdx.x >> 3;
    const int t = 8 * ((xcd >> 1) & 1) + (idx & 7), h = 8 * (xcd >> 2) + 4 * (xcd & 1) + (idx >> 3);
    const int j = (int)(0xdcfba9876e543210ull >> (4 * h)) & 15;
    const int tile = (xcd >> 1) * 56 + (xcd & 1) * 32 + idx, rt = tile / 7, ct = (xcd & 1) && idx >= 24 ? 7 : tile % 7;
    const int w = RFL(threadIdx.x >> 6);
    {
        const unsigned lane = threadIdx.x & 63, l0 = (unsigned)(uintptr_t)lds;
        const bool isx = w < 8;
        const unsigned pitch = isx ? DM * 2 : NQKV * 2, s0 = isx ? lane >> 4 : (lane >> 2) & 4;
        const unsigned v0 = (lane >> 3) * pitch + (((lane & 7) ^ s0) << 4), v1 = (v0 ^ (isx ? 64 : 0)) + 8 * pitch;
        const char* src = isx ? (const char*)P.x + (t * 128 + w * 16) * (DM * 2) : (const char*)P.wqkv + (w - 8) * 16 * (NQKV * 2) + (7 + (j < 14 ? j : 0)) * 128;
        const unsigned d0 = l0 + (isx ? w * 2048 : kv_off(0) + (w - 8) * 2048), d1 = d0 + (isx ? 16384 : kv_off(1) - kv_off(0));
        glds16s(v0, src, d0); glds16s(v1, src, d0 + 1024);
        src += isx ? 128 : 64 * NQKV * 2;
        glds16s(v0, src, d1); glds16s(v1, src, d1 + 1024);
    }
    const rsrc_t rs = __builtin_amdgcn_make_buffer_rsrc(P.ws, 0, WS_END, 0x00020000);
    if (w == 1) {
        const char* pc; asm volatile("s_getpc_b64 %0" : "=s"(pc));
        const int lane = lane_id();
        if (lane < 8) touch(pc + (idx * 8 + lane) * 128, (unsigned)(uintptr_t)lds + L_SINK);
    }
    if (w < 8) a_wave(P, rs, lds, h, j, t, w);
    else f_wave(P, rs, lds, h, j, t, rt, ct, w - 8);
}

extern "C" void kernel_launch(void* const* d_in, const int* in_sizes, int n_in, void* d_out, int out_size, void* d_ws, size_t ws_size,
                              hipStream_t stream) {
    Ptrs P;
    P.x = (const u16*)d_in[0]; P.wqkv = (const u16*)d_in[1]; P.wo = (const u16*)d_in[2];
    P.w1 = (const u16*)d_in[4]; P.w2 = (const u16*)d_in[5]; P.gw = (const u16*)d_in[6];
    P.out = (u16*)d_out; P.ws = (char*)d_ws;
    (void)hipFuncSetAttribute((const void*)fused_block, hipFuncAttributeMaxDynamicSharedMemorySize, L_TOTAL);
    fused_block<<<256, 768, L_TOTAL, stream>>>(P);
}
```
